```python
import torch

def _softmax(x, axis=-1):
    return torch.softmax(x, dim=axis)
def _gelu(x):
    return torch.nn.functional.gelu(x, approximate="tanh")
def _silu(x):
    return torch.nn.functional.silu(x)
def _sigmoid(x):
    return torch.sigmoid(x)
def _layernorm(x, axis=-1, eps=1e-5):
    m = x.mean(dim=axis, keepdim=True)
    v = x.var(dim=axis, keepdim=True, unbiased=False)
    return (x - m) / torch.sqrt(v + eps)
def _scatter_add(tgt, idx, src, axis=0):
    return tgt.clone().index_add_(axis, idx, src)
def _segment_sum(src, seg, n):
    out = torch.zeros((n,) + src.shape[1:], dtype=src.dtype)
    return out.index_add_(0, seg, src)
def _pad(x, pw):
    flat = tuple(v for pair in reversed(pw) for v in pair)
    return torch.nn.functional.pad(x, flat)

kernel_name = 'recurrent_scan__mha_block__glu_gated_glu__prompt'

def setup_inputs():

    _w = (lambda a: (lambda t: t.to(torch.bfloat16) if t.is_floating_point() else t)(torch.as_tensor(a)))
    def _A_setup():
        B, S, D, H = 1, 2048, 448, 16
        return {
            "x": _w((lambda *s: torch.randn(*s, dtype=torch.bfloat16) * 0.05)(B, S, D)),
            "wqkv": _w((lambda *s: torch.randn(*s, dtype=torch.bfloat16) * 0.05)(D, 3 * D)),
            "wo": _w((lambda *s: torch.randn(*s, dtype=torch.bfloat16) * 0.05)(D, D)),
        }
    _A_in = _A_setup()

    def _B_setup():
        B, S, D, F = 1, 2048, 448, 1792
        return {
            "x": _w((lambda *s: torch.randn(*s, dtype=torch.bfloat16) * 0.05)(B, S, D)),
            "w_gate": _w((lambda *s: torch.randn(*s, dtype=torch.bfloat16) * 0.05)(D, F)),
            "w_up": _w((lambda *s: torch.randn(*s, dtype=torch.bfloat16) * 0.05)(D, F)),
            "w_down": _w((lambda *s: torch.randn(*s, dtype=torch.bfloat16) * 0.05)(F, D)),
        }
    _B_in = _B_setup()

    return {
        "A_x": _A_in["x"],
        "A_wqkv": _A_in["wqkv"],
        "A_wo": _A_in["wo"],
        "B_x": _B_in["x"],
        "B_w_gate": _B_in["w_gate"],
        "B_w_up": _B_in["w_up"],
        "B_w_down": _B_in["w_down"],
    }

def reference(A_x, A_wqkv, A_wo, B_x, B_w_gate, B_w_up, B_w_down):
    def _A_ref(x, wqkv, wo):
        B, S, D = x.shape
        H = 16
        Dh = D // H
        qkv = (lambda x, *d: x.permute(*d))((x @ wqkv).reshape(B, S, 3, H, Dh), 2, 0, 3, 1, 4)
        q, k, v = qkv[0], qkv[1], qkv[2]
        scores = torch.einsum("bhqd,bhkd->bhqk", q, k) * (Dh ** -0.5)
        p = _softmax(scores, axis=-1)
        o = torch.einsum("bhqk,bhkd->bhqd", p, v)
        return (lambda x, *d: x.permute(*d))(o, 0, 2, 1, 3).reshape(B, S, D) @ wo

    def _B_ref(x, w_gate, w_up, w_down):
        g = torch.einsum("bsd,df->bsf", x, w_gate)
        u = torch.einsum("bsd,df->bsf", x, w_up)
        h = _sigmoid(g) * u
        return torch.einsum("bsf,fd->bsd", h, w_down)

    x = A_x
    xn = _layernorm(x, axis=-1)
    s = _A_ref(xn, A_wqkv, A_wo)
    g = _silu(_B_ref(xn, B_w_gate, B_w_up, B_w_down))
    return x + s * g
```

```cpp
#include <hip/hip_runtime.h>
#include <hip/hip_bf16.h>
#include <stdint.h>
#include <utility>

typedef short bf16x8 __attribute__((ext_vector_type(8)));
typedef short s16x4 __attribute__((ext_vector_type(4)));
typedef __bf16 bf16x2 __attribute__((ext_vector_type(2)));
typedef float f32x2 __attribute__((ext_vector_type(2)));
typedef float f32x4 __attribute__((ext_vector_type(4)));
typedef float f32x16 __attribute__((ext_vector_type(16)));
typedef unsigned u32x2 __attribute__((ext_vector_type(2)));
typedef unsigned u32x4 __attribute__((ext_vector_type(4)));
#define LAS __attribute__((address_space(3)))
#define DI __device__ __forceinline__
#define MFMA(a, b, c) __builtin_amdgcn_mfma_f32_32x32x16_bf16((a), (b), (c), 0, 0, 0)
#define SBAR() __builtin_amdgcn_sched_barrier(0)
#define PIN(x) asm volatile("" : "+v"(x))

constexpr int SEQ = 2048, DM = 448, NH = 16, DH = 28, FF = 1792;
constexpr size_t WS_Q = 0;
constexpr size_t WS_KV = 2u << 20;
constexpr size_t WS_H = 6u << 20;
constexpr size_t WS_O = 14u << 20;
constexpr size_t WS_G = 16u << 20;
constexpr float LOG2E = 1.4426950408889634f;
constexpr float QSCALE = 0.18898223650461362f * LOG2E;

DI unsigned cvtpk(float a, float b) { f32x2 v = {a, b}; bf16x2 r = __builtin_convertvector(v, bf16x2); return __builtin_bit_cast(unsigned, r); }
DI float bfround(float a) { return __uint_as_float(cvtpk(a, 0.f) << 16); }
DI float dot2(unsigned a, unsigned b, float c) { return __builtin_amdgcn_fdot2_f32_bf16(__builtin_bit_cast(bf16x2, a), __builtin_bit_cast(bf16x2, b), c, false); }
DI float swap32_add(float v) { auto r = __builtin_amdgcn_permlane32_swap(__float_as_uint(v), __float_as_uint(v), false, false); return __uint_as_float(r[0]) + __uint_as_float(r[1]); }
DI float swap32_max(float v) { auto r = __builtin_amdgcn_permlane32_swap(__float_as_uint(v), __float_as_uint(v), false, false); return fmaxf(__uint_as_float(r[0]), __uint_as_float(r[1])); }
DI float upper_half(float v) { auto r = __builtin_amdgcn_permlane32_swap(__float_as_uint(v), __float_as_uint(v), false, false); return __uint_as_float(r[1]); }
DI void glds16(const void* sbase, unsigned voff, unsigned lds) {
  unsigned keep;
  asm volatile("s_mov_b32 %0, m0\n\ts_mov_b32 m0, %3\n\ts_nop 0\n\tglobal_load_lds_dwordx4 %1, %2\n\ts_mov_b32 m0, %0"
               : "=&s"(keep) : "v"(voff), "s"(sbase), "s"(lds) : "memory");
}
typedef short v4i16_t __attribute__((ext_vector_type(4)));
DI s16x4 trread(unsigned a) { return __builtin_bit_cast(s16x4, __builtin_amdgcn_ds_read_tr16_b64_v4i16((LAS v4i16_t*)(uintptr_t)a)); }
DI bf16x8 trfrag(unsigned a0, unsigned a1) { s16x4 lo = trread(a0), hi = trread(a1); return __builtin_shufflevector(lo, hi, 0, 1, 2, 3, 4, 5, 6, 7); }
DI bf16x8 ldsfrag(unsigned a) { return *(const LAS bf16x8*)(uintptr_t)a; }
template <int N> DI void wait_vm() { asm volatile("s_waitcnt vmcnt(%0)" ::"n"(N) : "memory"); }
template <int N> DI void wait_vm_bar() { asm volatile("s_waitcnt vmcnt(%0) lgkmcnt(0)\n\ts_barrier" ::"n"(N) : "memory"); }
DI void wait_lds_bar() { asm volatile("s_waitcnt lgkmcnt(0)\n\ts_barrier" ::: "memory"); }
template <class F, int... I> DI void unroll_impl(F&& f, std::integer_sequence<int, I...>) { (f(std::integral_constant<int, I>{}), ...); }
template <int N, class F> DI void unroll(F&& f) { unroll_impl(f, std::make_integer_sequence<int, N>{}); }
DI bf16x8 pack8(const f32x16& v, int o) {
  u32x4 p = {cvtpk(v[o], v[o + 1]), cvtpk(v[o + 2], v[o + 3]), cvtpk(v[o + 4], v[o + 5]), cvtpk(v[o + 6], v[o + 7])};
  return __builtin_bit_cast(bf16x8, p);
}

constexpr int K1_SLOT = 8704, K1_NSLOT = 18;
constexpr int K1_CS = K1_SLOT * K1_NSLOT;
constexpr int K1_ST = K1_CS + 4096;
constexpr int K1_RS = K1_ST + 2048;
constexpr int K1_LDS = K1_RS + 512;
static_assert(K1_LDS <= 163840, "lds");
constexpr int k1_end(int st) { return st < 0 ? 0 : st < 7 ? 4 * (st + 1) : st < 14 ? 28 + 2 * (st - 6) : 42 + (st - 13); }
constexpr int k1_issued_after(int st, int tot) { int v = K1_NSLOT + k1_end(st - 1); return v < tot ? v : tot; }

template <bool ISV>
DI void k1_body(const char* x, const char* wA, const char* wB, const char* wC, int pitch, char* ws, int rb, int cg, unsigned lds0) {
  constexpr int TOT = ISV ? 42 : 49, NST = ISV ? 14 : 21;
  const int tid = threadIdx.x, lane = tid & 63, w = __builtin_amdgcn_readfirstlane(tid >> 6);
  const int tt = w & 3, ch = w >> 2, r = lane & 31, h = lane >> 5;
  const int xrow = 8 * w + (lane >> 3);
  const unsigned xvoff = xrow * (DM * 2) + (((lane & 7) ^ ((xrow >> 1) & 7)) << 4);
  const unsigned wvoff = ((lane >> 3) * 4 + tt) * pitch + ((lane & 7) << 4);
  const unsigned cvoff = ((lane >> 3) * 4 + tt + 32 * ch) * (3 * DM * 2) + ((lane & 7) << 4);
  auto issue = [&](auto U) {
    constexpr int u = U.value;
    constexpr unsigned slot = (u % K1_NSLOT) * K1_SLOT;
    if constexpr (u < 28) {
      constexpr int c = u >> 2, k = u & 3;
      if constexpr (k < 2) glds16(x + k * 64 * (DM * 2) + c * 128, xvoff, lds0 + slot + w * 1024);
      else glds16(wA + (size_t)(64 * c + 32 * (k - 2)) * pitch, wvoff, lds0 + slot + w * 1056);
    } else if constexpr (u < 42) {
      glds16(wB + (size_t)(32 * (u - 28)) * pitch, wvoff, lds0 + slot + w * 1056);
    } else {
      glds16(wC + (size_t)(64 * (u - 42)) * (3 * DM * 2), cvoff, lds0 + slot + w * 1088);
    }
  };
  unroll<K1_NSLOT>([&](auto U) { issue(U); });
  const int rot = tt + 2 * ch;
  const int g = (lane >> 4) & 1, qq = (lane & 15) >> 2, p = lane & 3;
  unsigned xa[4], wa[4], ca[4];
  {
    const int row = 32 * (tt & 1) + r;
#pragma unroll
    for (int i = 0; i < 4; ++i) {
      const int s = (i + rot) & 3;
      xa[i] = lds0 + (tt >> 1) * K1_SLOT + row * 128 + (((2 * s + h) ^ ((row >> 1) & 7)) << 4);
      wa[i] = lds0 + (s >> 1) * K1_SLOT + (4 * g + qq) * 1056 + (4 * (s & 1) + 2 * h) * 128 + ch * 64 + p * 8;
      ca[i] = lds0 + (4 * (s >> 1) + qq) * 1088 + (4 * (s & 1) + 2 * h) * 128 + ch * 64 + g * 32 + p * 8;
    }
  }
  bf16x8 xf[28];
  bf16x8 wf[2][2];
  f32x16 acc[2][2];
  float cs[2] = {0.f, 0.f}, sx = 0.f, sxx = 0.f;
  float rden = 0.f;
  bf16x8 mfrag = {};
  u32x4 outp = {};
  const int T = 4 * rb + tt;
  LAS float* csl = (LAS float*)(uintptr_t)(lds0 + K1_CS);
  LAS f32x2* stl = (LAS f32x2*)(uintptr_t)(lds0 + K1_ST);
  LAS float* rsl = (LAS float*)(uintptr_t)(lds0 + K1_RS);

  auto publish_cs = [&](int nt) {
#pragma unroll
    for (int tl = 0; tl < nt; ++tl) { csl[((2 * ch + tl) * 4 + tt) * 64 + lane] = cs[tl]; cs[tl] = 0.f; }
  };
  auto correct = [&](f32x16& a, int tl) {
    const LAS float* q = csl + (2 * ch + tl) * 256 + lane;
    const float c = (q[0] + q[64]) + (q[128] + q[192]);
    const float hi = bfround(c);
    u32x4 cf = {cvtpk(hi, c - hi), 0u, 0u, 0u};
    if constexpr (ISV) a = MFMA(mfrag, __builtin_bit_cast(bf16x8, cf), a);
    else a = MFMA(__builtin_bit_cast(bf16x8, cf), mfrag, a);
  };
  auto stats = [&]() {
    const f32x2 a = stl[32 * tt + r], b = stl[128 + 32 * tt + r];
    const float mean = (a[0] + b[0]) * (1.f / DM);
    const float var = fmaxf((a[1] + b[1]) * (1.f / DM) - mean * mean, 0.f);
    const float den = bfround(__builtin_sqrtf(bfround(bfround(var) + 1e-5f)));
    rden = 1.f / den;
    u32x4 mf = {cvtpk(-mean, -mean), 0u, 0u, 0u};
    mfrag = __builtin_bit_cast(bf16x8, mf);
    if (ISV && ch == 0 && h == 0) rsl[32 * tt + r] = rden;
  };
  auto glu2 = [&](const f32x16& a, int e) {
    const float sc = -LOG2E * rden;
    const float e0 = __builtin_amdgcn_exp2f(a[2 * e] * sc), e1 = __builtin_amdgcn_exp2f(a[2 * e + 1] * sc);
    const float h0 = a[8 + 2 * e] * rden * __builtin_amdgcn_rcpf(1.f + e0), h1 = a[9 + 2 * e] * rden * __builtin_amdgcn_rcpf(1.f + e1);
    outp[e] = cvtpk(h0, h1);
  };
  auto store_h = [&](int pass, int tl) {
    *(u32x4*)(ws + WS_H + (size_t)(T * 112 + 8 * cg + 4 * pass + 2 * ch + tl) * 1024 + lane * 16) = outp;
  };
  auto store_v = [&](const f32x16& a, int pass, int tl) {
    const int col = 256 * (cg - 14) + 128 * pass + 64 * g + 16 * (2 * ch + tl) + (lane & 15);
    const int head = (col * 2341) >> 16, d = col - 28 * head;
    f32x16 v;
#pragma unroll
    for (int a4 = 0; a4 < 4; ++a4) {
      const f32x4 rs = *(const LAS f32x4*)(rsl + 32 * tt + 8 * a4 + 4 * h);
#pragma unroll
      for (int j = 0; j < 4; ++j) v[4 * a4 + j] = a[4 * a4 + j] * rs[j];
    }
    if (col < DM) {
      char* dst = ws + WS_KV + (size_t)((head * 64 + T) * 4 + 2) * 1024 + (d + 32 * h) * 16;
      *(bf16x8*)dst = pack8(v, 0);
      *(bf16x8*)(dst + 1024) = pack8(v, 8);
      if (d >= 24) {
        const unsigned pv = d == 24 ? 0x3f803f80u : 0u;
        const u32x4 pad = {pv, pv, pv, pv};
        *(u32x4*)(dst + 64) = pad;
        *(u32x4*)(dst + 1024 + 64) = pad;
      }
    }
  };

  constexpr int NG = NST * 4;
  unroll<NG + 1>([&](auto GG) {
    constexpr int G = GG.value, st = G >> 2, i = G & 3;
    if constexpr (G < NG) {
      if constexpr (i == 0) {
        constexpr int before = st == 0 ? K1_NSLOT : k1_issued_after(st - 1, TOT);
        wait_vm_bar<before - k1_end(st)>();
        unroll<k1_issued_after(st, TOT) - before>([&](auto U) { issue(std::integral_constant<int, before + U.value>{}); });
      }
      constexpr int u0 = k1_end(st - 1);
      if constexpr (st < 7) {
        xf[G] = ldsfrag(xa[i] + (u0 % K1_NSLOT) * K1_SLOT);
        constexpr unsigned o = ((u0 + 2) % K1_NSLOT) * K1_SLOT;
        wf[G & 1][0] = trfrag(wa[i] + o, wa[i] + o + 128);
        wf[G & 1][1] = trfrag(wa[i] + o + 32, wa[i] + o + 160);
      } else if constexpr (st < 14) {
        constexpr unsigned o = (u0 % K1_NSLOT) * K1_SLOT;
        wf[G & 1][0] = trfrag(wa[i] + o, wa[i] + o + 128);
        wf[G & 1][1] = trfrag(wa[i] + o + 32, wa[i] + o + 160);
      } else {
        constexpr unsigned o = (u0 % K1_NSLOT) * K1_SLOT;
        wf[G & 1][0] = trfrag(ca[i] + o, ca[i] + o + 128);
      }
      SBAR();
    }
    if constexpr (G > 0) {
      constexpr int M = G - 1, pass = M / 28, ks = M % 28, nt = pass < 2 ? 2 : 1;
      const f32x16 zero = {};
#pragma unroll
      for (int tl = 0; tl < nt; ++tl) {
        const f32x16 cin = ks == 0 ? zero : acc[pass & 1][tl];
        if constexpr (ISV) acc[pass & 1][tl] = MFMA(xf[ks], wf[M & 1][tl], cin);
        else acc[pass & 1][tl] = MFMA(wf[M & 1][tl], xf[ks], cin);
      }
      if constexpr ((M & 3) == 0) {
#pragma unroll
        for (int tl = 0; tl < nt; ++tl) {
          const u32x4 v = __builtin_bit_cast(u32x4, wf[M & 1][tl]);
#pragma unroll
          for (int j = 0; j < 4; ++j) { const unsigned e = v[j]; cs[tl] = dot2(e, 0x3f803f80u, cs[tl]); }
        }
      }
      if constexpr (pass == 0 && (M & 3) < 2) {
        const u32x4 v = __builtin_bit_cast(u32x4, xf[ks]);
#pragma unroll
        for (int j = 0; j < 4; ++j) { const unsigned e = v[j]; sx = dot2(e, 0x3f803f80u, sx); sxx = dot2(e, e, sxx); }
      }
      if constexpr (ks == 25) {
        publish_cs(nt);
        if constexpr (pass == 0) {
          const f32x2 s2 = {swap32_add(sx), swap32_add(sxx)};
          if (h == 0) stl[128 * ch + 32 * tt + r] = s2;
        }
      }
      if constexpr (pass > 0) {
        constexpr int pp = pass - 1;
        if constexpr (ks == 1) {
          if constexpr (pp == 0) stats();
          correct(acc[pp & 1][0], 0);
          correct(acc[pp & 1][1], 1);
        }
        if constexpr (!ISV) {
          if constexpr (ks >= 4 && ks < 20 && (ks & 1) == 0) {
            constexpr int e = (ks - 4) >> 1;
            glu2(acc[pp & 1][e >> 2], e & 3);
            if constexpr ((e & 3) == 3) store_h(pp, e >> 2);
          }
        } else {
          if constexpr (ks == 6) store_v(acc[pp & 1][0], pp, 0);
          if constexpr (ks == 12) store_v(acc[pp & 1][1], pp, 1);
        }
      }
      SBAR();
    }
  });
  wait_lds_bar();
  if constexpr (ISV) {
    correct(acc[1][0], 0);
    correct(acc[1][1], 1);
    store_v(acc[1][0], 1, 0);
    store_v(acc[1][1], 1, 1);
  } else {
    correct(acc[0][0], 0);
    const bool isk = cg >= 7;
    const float sc = isk ? rden : rden * QSCALE;
#pragma unroll
    for (int a4 = 0; a4 < 4; ++a4) {
      const int Q = 16 * (isk ? cg - 7 : cg) + 8 * ch + 2 * a4 + h;
      const int head = (Q * 147) >> 10, dq = Q - 7 * head;
      char* dst = ws + (isk ? WS_KV + (size_t)(head * 64 + T) * 4096 : WS_Q + (size_t)(head * 64 + T) * 2048) +
                  (dq >> 2) * 1024 + (r + 32 * (dq & 1)) * 16 + ((dq >> 1) & 1) * 8;
      const f32x16& a = acc[0][0];
      const u32x2 v = {cvtpk(a[4 * a4] * sc, a[4 * a4 + 1] * sc), cvtpk(a[4 * a4 + 2] * sc, a[4 * a4 + 3] * sc)};
      *(u32x2*)dst = v;
      if (dq == 6) { const u32x2 pad = {isk ? 0x3f80u : 0u, 0u}; *(u32x2*)(dst + 512) = pad; }
    }
  }
}

__global__ __launch_bounds__(512, 2) void k1_proj(const char* x, const char* wqkv, const char* wg, const char* wu, char* ws) {
  extern __shared__ __attribute__((aligned(16))) char smem[];
  const unsigned lds0 = (unsigned)(uintptr_t)smem;
  const int xcd = blockIdx.x & 7, idx = blockIdx.x >> 3;
  const int rb = (xcd & 1) * 8 + (idx & 7), cg = (xcd >> 1) * 4 + (idx >> 3);
  const int b = __builtin_amdgcn_readfirstlane(threadIdx.x >> 8);
  const char* xb = x + (size_t)rb * 128 * (DM * 2);
  if (cg < 14) {
    const char* wm = (b ? wu : wg) + cg * 256;
    k1_body<false>(xb, wm, wm + 128, wqkv + cg * 128, FF * 2, ws, rb, cg, lds0);
  } else {
    const char* wv = wqkv + 2 * DM * 2 + (cg - 14) * 512;
    k1_body<true>(xb, wv + b * 128, wv + 256 + (cg == 14 ? b * 128 : 0), wqkv, 3 * DM * 2, ws, rb, cg, lds0);
  }
}

constexpr int K2_RING = 6;
constexpr int K2_GW = K2_RING * 4096;
constexpr int K2_AM = 4 * K2_GW;
constexpr int K2_CNT = K2_AM + 4 * 3 * 4096;
constexpr int K2_LDS = K2_CNT + 16;
static_assert(K2_LDS <= 163840, "lds");

DI void put_tile(unsigned a, const f32x16& v) {
#pragma unroll
  for (int j = 0; j < 4; ++j) { const f32x4 t = {v[4 * j], v[4 * j + 1], v[4 * j + 2], v[4 * j + 3]}; *(LAS f32x4*)(uintptr_t)(a + j * 1024) = t; }
}
DI void add_tile(unsigned a, f32x16& v) {
#pragma unroll
  for (int j = 0; j < 4; ++j) { const f32x4 t = *(const LAS f32x4*)(uintptr_t)(a + j * 1024);
#pragma unroll
    for (int e = 0; e < 4; ++e) v[4 * j + e] += t[e]; }
}
DI float rowmax16(const f32x16& s) {
  float a = fmaxf(fmaxf(s[0], s[1]), s[2]), b = fmaxf(fmaxf(s[3], s[4]), s[5]);
  a = fmaxf(fmaxf(a, s[6]), s[7]); b = fmaxf(fmaxf(b, s[8]), s[9]);
  a = fmaxf(fmaxf(a, s[10]), s[11]); b = fmaxf(fmaxf(b, s[12]), s[13]);
  a = fmaxf(fmaxf(a, s[14]), s[15]);
  return swap32_max(fmaxf(a, b));
}

DI void k2_attn(char* ws, int head, int qb, int w, unsigned lds0) {
  const int lane = threadIdx.x & 63, r = lane & 31, h = lane >> 5;
  const bf16x8* kv = (const bf16x8*)(ws + WS_KV + (size_t)head * 64 * 4096) + lane;
  const bf16x8* qp = (const bf16x8*)(ws + WS_Q + (size_t)(head * 64 + 4 * qb) * 2048) + lane;
  const f32x16 zero = {};
  bf16x8 q[4][2];
#pragma unroll
  for (int j = 0; j < 4; ++j) { const int t = (w + 1 + j) & 3; q[j][0] = qp[t * 128]; q[j][1] = qp[t * 128 + 64]; }
  const bf16x8 r0 = kv[0], r1 = kv[64];
  bf16x8 kb[3][4];
  const bf16x8* kw = kv + (size_t)w * 16 * 256;
#pragma unroll
  for (int b = 0; b < 2; ++b)
#pragma unroll
    for (int f = 0; f < 4; ++f) kb[b][f] = kw[b * 256 + f * 64];
  asm volatile("s_barrier" ::: "memory");
#pragma unroll
  for (int j = 0; j < 4; ++j) {
    u32x4 qq = __builtin_bit_cast(u32x4, q[j][1]);
    if (h) { qq[2] = 0u; qq[3] = 0u; }
    f32x16 s = MFMA(r0, q[j][0], zero);
    s = MFMA(r1, __builtin_bit_cast(bf16x8, qq), s);
    const float m = rowmax16(s);
    if (h) qq[2] = cvtpk(-m, 0.f);
    q[j][1] = __builtin_bit_cast(bf16x8, qq);
  }
  f32x16 o[4] = {zero, zero, zero, zero};
  f32x16 sA, sB;
  sA = MFMA(kb[0][0], q[0][0], zero); sA = MFMA(kb[0][1], q[0][1], sA);
#define EX4(S, i) do { S[i] = __builtin_amdgcn_exp2f(S[i]); S[i + 1] = __builtin_amdgcn_exp2f(S[i + 1]); S[i + 2] = __builtin_amdgcn_exp2f(S[i + 2]); S[i + 3] = __builtin_amdgcn_exp2f(S[i + 3]); } while (0)
#define ASTEP(C, N, J, KB, KN, JN) do { bf16x8 p0, p1; \
    N = MFMA(KN[0], q[JN][0], zero); SBAR(); EX4(C, 0); SBAR(); \
    N = MFMA(KN[1], q[JN][1], N); SBAR(); EX4(C, 4); p0 = pack8(C, 0); PIN(p0); SBAR(); \
    EX4(C, 8); SBAR(); o[J] = MFMA(KB[2], p0, o[J]); SBAR(); EX4(C, 12); p1 = pack8(C, 8); PIN(p1); SBAR(); \
    o[J] = MFMA(KB[3], p1, o[J]); SBAR(); } while (0)
#define ATILE(B, BN, BL, t) do { \
    { const int tn = (t) + 2 < 16 ? (t) + 2 : 15; _Pragma("unroll") for (int f = 0; f < 4; ++f) kb[BL][f] = kw[tn * 256 + f * 64]; SBAR(); } \
    ASTEP(sA, sB, 0, kb[B], kb[B], 1); ASTEP(sB, sA, 1, kb[B], kb[B], 2); ASTEP(sA, sB, 2, kb[B], kb[B], 3); ASTEP(sB, sA, 3, kb[B], kb[BN], 0); } while (0)
  for (int t = 0; t < 15; t += 3) { ATILE(0, 1, 2, t); ATILE(1, 2, 0, t + 1); ATILE(2, 0, 1, t + 2); }
  ATILE(0, 1, 2, 15);
#undef ATILE
#undef ASTEP
  const unsigned am = lds0 + K2_AM + lane * 16;
#pragma unroll
  for (int j = 0; j < 3; ++j) put_tile(am + (w * 3 + j) * 4096, o[j]);
  wait_lds_bar();
  f32x16 a = o[3];
#pragma unroll
  for (int j = 0; j < 3; ++j) add_tile(am + ((((w + 3 - j) & 3) * 3) + j) * 4096, a);
  float l = upper_half(a[12]);
  float rl = 1.f / l;
  bool bad = !(l < 3.0e38f);
#pragma unroll
  for (int i = 0; i < 16; ++i) { a[i] *= rl; bad |= !(fabsf(a[i]) < 3.0e38f); }
  if (__any(bad)) {
    u32x4 qq = __builtin_bit_cast(u32x4, q[3][1]);
    if (h) qq[2] = 0u;
    const bf16x8 q1 = __builtin_bit_cast(bf16x8, qq);
    float m = -3.0e38f;
    a = zero;
    for (int t = 0; t < 64; ++t) {
      const bf16x8 k0 = kv[t * 256], k1 = kv[t * 256 + 64], v0 = kv[t * 256 + 128], v1 = kv[t * 256 + 192];
      f32x16 s = MFMA(k0, q[3][0], zero);
      s = MFMA(k1, q1, s);
      const float mn = fmaxf(m, rowmax16(s));
      const float al = __builtin_amdgcn_exp2f(m - mn);
      m = mn;
#pragma unroll
      for (int i = 0; i < 16; ++i) { a[i] *= al; s[i] = __builtin_amdgcn_exp2f(s[i] - mn); }
      a = MFMA(v0, pack8(s, 0), a);
      a = MFMA(v1, pack8(s, 8), a);
    }
    l = upper_half(a[12]);
    rl = 1.f / l;
#pragma unroll
    for (int i = 0; i < 16; ++i) a[i] *= rl;
  }
  char* ob = ws + WS_O + (size_t)(4 * qb + w) * 28 * 1024;
#pragma unroll
  for (int a4 = 0; a4 < 4; ++a4) {
    const int dq = 2 * a4 + h, Q = 7 * head + dq;
    const u32x2 v = {cvtpk(a[4 * a4], a[4 * a4 + 1]), cvtpk(a[4 * a4 + 2], a[4 * a4 + 3])};
    if (dq < 7) *(u32x2*)(ob + (Q >> 2) * 1024 + (r + 32 * (Q & 1)) * 16 + ((Q >> 1) & 1) * 8) = v;
  }
}

DI void k2_down(const char* wd, char* ws, int tp, int np, int gw, unsigned lds0) {
  const int lane = threadIdx.x & 63, h = lane >> 5;
  const unsigned ring = lds0 + gw * K2_GW;
  const char* wsrc = wd + (size_t)(gw * 28 * 16) * (DM * 2) + np * 128;
  const char* hsrc = ws + WS_H + (size_t)(2 * tp * 112 + gw * 28) * 1024;
  const int wr = lane >> 3;
  const unsigned wvoff = wr * (DM * 2) + ((((lane >> 2) & 1) ^ ((wr >> 1) & 1)) << 6) + ((lane & 3) << 4);
  const unsigned hvoff = lane * 16;
  auto issue = [&](int ks, int slot) {
    const unsigned d = ring + slot * 4096;
    glds16(wsrc + (size_t)ks * 16 * (DM * 2), wvoff, d);
    glds16(wsrc + (size_t)(ks * 16 + 8) * (DM * 2), wvoff, d + 1024);
    glds16(hsrc + (size_t)ks * 1024, hvoff, d + 2048);
    glds16(hsrc + (size_t)(ks + 112) * 1024, hvoff, d + 3072);
  };
#pragma unroll
  for (int s = 0; s < K2_RING - 1; ++s) issue(s, s);
  asm volatile("s_barrier" ::: "memory");
  const int g = (lane >> 4) & 1, qq = (lane & 15) >> 2, p = lane & 3;
  const unsigned wa0 = ring + (4 * h + qq) * 128 + (((qq >> 1) & 1) << 6) + g * 32 + p * 8, wa1 = wa0 ^ 64;
  const unsigned ha = ring + 2048 + lane * 16;
  const f32x16 zero = {};
  f32x16 acc[4] = {zero, zero, zero, zero};
  unsigned aw[2], ah[2];
  {
    const int t0 = (gw + 1) & 3;
    aw[0] = (t0 & 1) ? wa1 : wa0; aw[1] = (t0 & 1) ? wa0 : wa1;
    ah[0] = ha + ((t0 >> 1) & 1) * 1024; ah[1] = ha + (((t0 >> 1) & 1) ^ 1) * 1024;
  }
  const bool odd = (gw + 1) & 1;
  int slot = 0;
  for (int ks = 0; ks < 28; ++ks) {
    if (ks + K2_RING - 1 < 28) { wait_vm<4 * (K2_RING - 2)>(); } else { wait_vm<0>(); }
    const unsigned so = slot * 4096;
    const bf16x8 w0 = trfrag(aw[0] + so, aw[0] + so + 1024), w1 = trfrag(aw[1] + so, aw[1] + so + 1024);
    const bf16x8 h0 = ldsfrag(ah[0] + so), h1 = ldsfrag(ah[1] + so);
    if (ks + K2_RING - 1 < 28) issue(ks + K2_RING - 1, slot == 0 ? K2_RING - 1 : slot - 1);
    acc[0] = MFMA(w0, h0, acc[0]);
    acc[2] = MFMA(w0, h1, acc[2]);
    if (odd) { acc[1] = MFMA(w1, h1, acc[1]); acc[3] = MFMA(w1, h0, acc[3]); }
    else { acc[1] = MFMA(w1, h0, acc[1]); acc[3] = MFMA(w1, h1, acc[3]); }
    slot = slot == K2_RING - 1 ? 0 : slot + 1;
  }
#pragma unroll
  for (int j = 0; j < 3; ++j) put_tile(ring + lane * 16 + j * 4096, acc[j]);
  LAS unsigned* cnt = (LAS unsigned*)(uintptr_t)(lds0 + K2_CNT);
  asm volatile("s_waitcnt lgkmcnt(0)" ::: "memory");
  if (lane == 0) __hip_atomic_fetch_add(cnt, 1u, __ATOMIC_RELAXED, __HIP_MEMORY_SCOPE_WORKGROUP);
  while (__hip_atomic_load(cnt, __ATOMIC_RELAXED, __HIP_MEMORY_SCOPE_WORKGROUP) < 4u) __builtin_amdgcn_s_sleep(1);
  asm volatile("" ::: "memory");
  f32x16 a = acc[3];
#pragma unroll
  for (int j = 0; j < 3; ++j) add_tile(lds0 + ((gw + 3 - j) & 3) * K2_GW + lane * 16 + j * 4096, a);
#pragma unroll
  for (int i = 0; i < 16; ++i) a[i] = a[i] * __builtin_amdgcn_rcpf(1.f + __builtin_amdgcn_exp2f(-LOG2E * a[i]));
  char* dst = ws + WS_G + (size_t)((2 * tp + (gw >> 1)) * 14 + 2 * np + (gw & 1)) * 2048 + lane * 16;
  *(bf16x8*)dst = pack8(a, 0);
  *(bf16x8*)(dst + 1024) = pack8(a, 8);
}

__global__ __launch_bounds__(512, 2) void k2_attn_down(const char* wd, char* ws) {
  extern __shared__ __attribute__((aligned(16))) char smem[];
  const unsigned lds0 = (unsigned)(uintptr_t)smem;
  const int xcd = blockIdx.x & 7, idx = blockIdx.x >> 3;
  const int w = __builtin_amdgcn_readfirstlane(threadIdx.x >> 6);
  if (w < 4) {
    if (threadIdx.x == 0) { *(LAS unsigned*)(uintptr_t)(lds0 + K2_CNT) = 0u; asm volatile("s_waitcnt lgkmcnt(0)" ::: "memory"); }
    __builtin_amdgcn_s_setprio(1);
    k2_attn(ws, xcd * 2 + (idx >> 4), idx & 15, w, lds0);
  } else if (idx < 28) {
    k2_down(wd, ws, xcd * 4 + idx / 7, idx % 7, w - 4, lds0);
  }
}

constexpr int K3_LDS = 28 * 4096;
__global__ __launch_bounds__(256, 1) void k3_out(const char* x, const char* wo, const char* ws, char* out) {
  extern __shared__ __attribute__((aligned(16))) char smem[];
  const unsigned lds0 = (unsigned)(uintptr_t)smem;
  const int xcd = blockIdx.x & 7, idx = blockIdx.x >> 3;
  const int tp = xcd * 4 + idx / 7, np = idx % 7;
  const int lane = threadIdx.x & 63, w = __builtin_amdgcn_readfirstlane(threadIdx.x >> 6), r = lane & 31, h = lane >> 5;
  const int wr = lane >> 3;
  const unsigned wvoff = wr * (DM * 2) + ((((lane >> 2) & 1) ^ ((wr >> 1) & 1)) << 6) + ((lane & 3) << 4);
  const unsigned voff = w < 2 ? wvoff : lane * 16;
  const char* src = w < 2 ? wo + np * 128 + w * 8 * (DM * 2) : ws + WS_O + (size_t)(2 * tp + (w - 2)) * 28 * 1024;
  const int step = w < 2 ? 16 * DM * 2 : 1024;
#pragma unroll
  for (int ks = 0; ks < 28; ++ks) glds16(src + ks * step, voff, lds0 + ks * 4096 + w * 1024);
  const int T = 2 * tp + (w >> 1), NT = 2 * np + (w & 1);
  const size_t xo = (size_t)(32 * T + r) * (DM * 2) + NT * 64 + h * 8;
  u32x2 xv[4]; u32x4 gv[2];
  asm volatile("global_load_dwordx2 %0, %4, off\n\tglobal_load_dwordx2 %1, %4, off offset:16\n\tglobal_load_dwordx2 %2, %4, off offset:32\n\tglobal_load_dwordx2 %3, %4, off offset:48"
               : "=&v"(xv[0]), "=&v"(xv[1]), "=&v"(xv[2]), "=&v"(xv[3]) : "v"(x + xo) : "memory");
  asm volatile("global_load_dwordx4 %0, %2, off\n\tglobal_load_dwordx4 %1, %2, off offset:1024"
               : "=&v"(gv[0]), "=&v"(gv[1]) : "v"(ws + WS_G + (size_t)(T * 14 + NT) * 2048 + lane * 16) : "memory");
  const int g = (lane >> 4) & 1, qq = (lane & 15) >> 2, p = lane & 3;
  const unsigned wa = lds0 + (4 * h + qq) * 128 + ((((qq >> 1) & 1) ^ (w & 1)) << 6) + g * 32 + p * 8;
  const unsigned ha = lds0 + 2048 + (w >> 1) * 1024 + lane * 16;
  f32x16 acc = {};
  unroll<4>([&](auto C) {
    constexpr int c = C.value;
    wait_vm_bar<6 + 21 - 7 * c>();
#pragma unroll
    for (int k = 0; k < 7; ++k) {
      const unsigned so = (7 * c + k) * 4096;
      acc = MFMA(trfrag(wa + so, wa + so + 1024), ldsfrag(ha + so), acc);
    }
  });
  asm volatile("s_waitcnt vmcnt(0)" : "+v"(xv[0]), "+v"(xv[1]), "+v"(xv[2]), "+v"(xv[3]), "+v"(gv[0]), "+v"(gv[1])::"memory");
#pragma unroll
  for (int a4 = 0; a4 < 4; ++a4) {
    float y[4];
#pragma unroll
    for (int j = 0; j < 4; ++j) {
      const unsigned xw = xv[a4][j >> 1], gw = gv[a4 >> 1][2 * (a4 & 1) + (j >> 1)];
      const float xf = __uint_as_float((j & 1) ? (xw & 0xffff0000u) : (xw << 16));
      const float gf = __uint_as_float((j & 1) ? (gw & 0xffff0000u) : (gw << 16));
      y[j] = xf + acc[4 * a4 + j] * gf;
    }
    const u32x2 v = {cvtpk(y[0], y[1]), cvtpk(y[2], y[3])};
    *(u32x2*)(out + xo + a4 * 16) = v;
  }
}

extern "C" void kernel_launch(void* const* d_in, const int* in_sizes, int n_in, void* d_out, int out_size, void* d_ws, size_t ws_size,
                              hipStream_t stream) {
  const char* x = (const char*)d_in[0];
  const char* wqkv = (const char*)d_in[1];
  const char* wo = (const char*)d_in[2];
  const char* wg = (const char*)d_in[4];
  const char* wu = (const char*)d_in[5];
  const char* wd = (const char*)d_in[6];
  char* ws = (char*)d_ws;
  (void)hipFuncSetAttribute((const void*)k1_proj, hipFuncAttributeMaxDynamicSharedMemorySize, K1_LDS);
  (void)hipFuncSetAttribute((const void*)k2_attn_down, hipFuncAttributeMaxDynamicSharedMemorySize, K2_LDS);
  (void)hipFuncSetAttribute((const void*)k3_out, hipFuncAttributeMaxDynamicSharedMemorySize, K3_LDS);
  hipLaunchKernelGGL(k1_proj, dim3(256), dim3(512), K1_LDS, stream, x, wqkv, wg, wu, ws);
  hipLaunchKernelGGL(k2_attn_down, dim3(256), dim3(512), K2_LDS, stream, wd, ws);
  hipLaunchKernelGGL(k3_out, dim3(224), dim3(256), K3_LDS, stream, x, wo, (const char*)ws, (char*)d_out);
}
```

```cpp
#include <hip/hip_runtime.h>
#include <hip/hip_bf16.h>
#include <stdint.h>
#include <utility>

#if defined(__HIP_DEVICE_COMPILE__)
#pragma clang attribute push(__attribute__((target("no-packed-fp32-ops"))), apply_to = function)
#endif
typedef short bf16x8 __attribute__((ext_vector_type(8)));
typedef short s16x4 __attribute__((ext_vector_type(4)));
typedef __bf16 bf16x2 __attribute__((ext_vector_type(2)));
typedef float f32x2 __attribute__((ext_vector_type(2)));
typedef float f32x4 __attribute__((ext_vector_type(4)));
typedef float f32x16 __attribute__((ext_vector_type(16)));
typedef unsigned u32x2 __attribute__((ext_vector_type(2)));
typedef unsigned u32x4 __attribute__((ext_vector_type(4)));
#define LAS __attribute__((address_space(3)))
#define DI __device__ __forceinline__
#define MFMA(a, b, c) __builtin_amdgcn_mfma_f32_32x32x16_bf16((a), (b), (c), 0, 0, 0)
#define SBAR() __builtin_amdgcn_sched_barrier(0)
#define PIN(x) asm volatile("" : "+v"(x))

constexpr int SEQ = 2048, DM = 448, NH = 16, DH = 28, FF = 1792;
constexpr size_t WS_Q = 18u << 20;
constexpr size_t WS_KV = 2u << 20;
constexpr size_t WS_H = 6u << 20;
constexpr size_t WS_O = 14u << 20;
constexpr size_t WS_G = 16u << 20;
constexpr float LOG2E = 1.4426950408889634f;
constexpr float QSCALE = 0.18898223650461362f * LOG2E;

#define STAMP_DECL do {} while (0)
#define STAMP(slot) do {} while (0)
#define STAMP_FLUSH(ws, kern) do {} while (0)
DI float asf(unsigned u) { return __builtin_bit_cast(float, u); }
DI unsigned asu(float f) { return __builtin_bit_cast(unsigned, f); }
DI unsigned cvtpk(float a, float b) { f32x2 v = {a, b}; bf16x2 r = __builtin_convertvector(v, bf16x2); return __builtin_bit_cast(unsigned, r); }
DI float bfround(float a) { return asf(cvtpk(a, 0.f) << 16); }
DI float swap32_add(float v) { auto r = __builtin_amdgcn_permlane32_swap(asu(v), asu(v), false, false); return asf(r[0]) + asf(r[1]); }
DI float swap32_max(float v) { auto r = __builtin_amdgcn_permlane32_swap(asu(v), asu(v), false, false); return __builtin_fmaxf(asf(r[0]), asf(r[1])); }
DI float upper_half(float v) { auto r = __builtin_amdgcn_permlane32_swap(asu(v), asu(v), false, false); return asf(r[1]); }
DI void glds16(const void* sbase, unsigned voff, unsigned lds) {
  unsigned keep;
  asm volatile("s_mov_b32 %0, m0\n\ts_mov_b32 m0, %3\n\ts_nop 0\n\tglobal_load_lds_dwordx4 %1, %2\n\ts_mov_b32 m0, %0"
               : "=&s"(keep) : "v"(voff), "s"(sbase), "s"(lds) : "memory");
}
DI u32x4 make_srd(const void* p, unsigned bytes) { return u32x4{(unsigned)(uintptr_t)p, (unsigned)((uintptr_t)p >> 32) & 0xffffu, bytes, 0x00020000u}; }
template <int SOFF, int LOFF> DI void dma_c(u32x4 srd, unsigned voff, unsigned lds) {
  unsigned tmp;
  asm volatile("s_add_u32 m0, %3, %5\n\ts_mov_b32 %0, %4\n\tbuffer_load_dwordx4 %1, %2, %0 offen lds"
               : "=&s"(tmp) : "v"(voff), "s"(srd), "s"(lds), "n"(SOFF), "n"(LOFF) : "memory", "scc");
}
DI void set_m0(unsigned lds) { asm volatile("s_mov_b32 m0, %0\n\ts_nop 0" ::"s"(lds) : "memory"); }
template <int OFF> DI void dma_m0(u32x4 srd, unsigned voff, unsigned soff) {
  asm volatile("buffer_load_dwordx4 %0, %1, %2 offen offset:%3 lds" ::"v"(voff), "s"(srd), "s"(soff), "n"(OFF) : "memory");
}
typedef short v4i16_t __attribute__((ext_vector_type(4)));
DI s16x4 trread(unsigned a) { return __builtin_bit_cast(s16x4, __builtin_amdgcn_ds_read_tr16_b64_v4i16((LAS v4i16_t*)(uintptr_t)a)); }
DI bf16x8 trfrag(unsigned a0, unsigned a1) { s16x4 lo = trread(a0), hi = trread(a1); return __builtin_shufflevector(lo, hi, 0, 1, 2, 3, 4, 5, 6, 7); }
DI bf16x8 ldsfrag(unsigned a) { return *(const LAS bf16x8*)(uintptr_t)a; }
template <int N> DI void wait_vm() { asm volatile("s_waitcnt vmcnt(%0)" ::"n"(N) : "memory"); }
template <int N> DI void wait_vm_bar() { asm volatile("s_waitcnt vmcnt(%0) lgkmcnt(0)\n\ts_barrier" ::"n"(N) : "memory"); }
template <int N> DI void wait_vm_rawbar() { asm volatile("s_waitcnt vmcnt(%0)\n\ts_barrier" ::"n"(N) : "memory"); }
DI void wait_lds_bar() { asm volatile("s_waitcnt lgkmcnt(0)\n\ts_barrier" ::: "memory"); }
template <class F, int... I> DI void unroll_impl(F&& f, std::integer_sequence<int, I...>) { (f(std::integral_constant<int, I>{}), ...); }
template <int N, class F> DI void unroll(F&& f) { unroll_impl(f, std::make_integer_sequence<int, N>{}); }
DI bf16x8 pack8(const f32x16& v, int o) {
  u32x4 p = {cvtpk(v[o], v[o + 1]), cvtpk(v[o + 2], v[o + 3]), cvtpk(v[o + 4], v[o + 5]), cvtpk(v[o + 6], v[o + 7])};
  return __builtin_bit_cast(bf16x8, p);
}

typedef __bf16 bf16v8 __attribute__((ext_vector_type(8)));
typedef __bf16 bf16v16 __attribute__((ext_vector_type(16)));
typedef float f32x8 __attribute__((ext_vector_type(8)));
template <int O> DI f32x16 pv_mfma(bf16x8 v, const f32x16& p, f32x16 o, unsigned idx) {
  const f32x8 b = __builtin_shufflevector(p, p, O, O + 1, O + 2, O + 3, O + 4, O + 5, O + 6, O + 7);
  return __builtin_amdgcn_smfmac_f32_32x32x32_bf16(__builtin_bit_cast(bf16v8, v), __builtin_bit_cast(bf16v16, b), o, idx, 0, 0);
}
#define ST_AUX 16
typedef __amdgpu_buffer_rsrc_t rsrc_t;
DI rsrc_t ws_rsrc(char* ws) { return __builtin_amdgcn_make_buffer_rsrc(ws, 0, 32u << 20, 0x00020000); }
DI void st16(rsrc_t r, unsigned off, u32x4 v) { __builtin_amdgcn_raw_buffer_store_b128(v, r, off, 0, ST_AUX); }
DI void st16(rsrc_t r, unsigned off, bf16x8 v) { __builtin_amdgcn_raw_buffer_store_b128(__builtin_bit_cast(u32x4, v), r, off, 0, ST_AUX); }
DI void st8(rsrc_t r, unsigned off, u32x2 v, unsigned soff = 0) { __builtin_amdgcn_raw_buffer_store_b64(v, r, off, soff, ST_AUX); }

constexpr int K1_SLOT = 8704, K1_NSLOT = 18, K1_WIN = 6 * K1_SLOT;
constexpr int K1_CS = K1_SLOT * K1_NSLOT;
constexpr int K1_ST = K1_CS + 5120;
constexpr int K1_LDS = K1_ST + 2048;
static_assert(K1_LDS <= 163840, "lds");
constexpr unsigned OOB = 0x80000000u;
template <bool ISV> struct K1Plan {
  static constexpr int PS = ISV ? 4 : 5, BU = 7 * PS, TOT = BU + 14, P0 = PS + 3, LB = 3, NG = 84;
  static constexpr int need(int st) {
    return st < 7 ? PS * (st + 1) : st == 7 ? BU + 6 : st == 10 ? TOT : st == 14 ? 0 : -1;
  }
  static constexpr int bar_step(int st) { return st == 0 ? 0 : st <= 7 ? 4 * st - 1 : 4 * st - LB; }
  static constexpr int stores_at(int M) {
    if (M == 29) return 1;
    if (ISV) return (M == 28 + 14 || M == 28 + 20 || M == 56 + 6) ? 2 : 0;
    return (M == 35 || M == 37 || M == 39 || M == 41) ? 1 : 0;
  }
  struct Tab { int virt[TOT], before[NG + 1]; };
  static constexpr Tab make() {
    Tab t = {};
    int v = -1;
    for (int u = 0; u < TOT; ++u) {
      const bool pair = u < BU ? (u % PS == 0 || u % PS == 2) : ((u - BU) & 1) == 0;
      ++v; if (pair && v % K1_NSLOT == K1_NSLOT - 1) ++v;
      t.virt[u] = v;
    }
    int n = P0;
    for (int m = 0; m <= NG; ++m) {
      t.before[m] = n;
      const int sb = (m + 1) >> 2, consumed = sb < 7 ? PS * sb : BU;
      int q = m < 4 ? 2 : (!ISV && (m & 3) == 0) ? 2 : 1;
      while (q-- > 0 && n < TOT && t.virt[consumed] + K1_NSLOT > t.virt[n]) ++n;
    }
    return t;
  }
  static constexpr Tab tab = make();
  static constexpr int slot(int u) { return (tab.virt[u] % K1_NSLOT) * K1_SLOT; }
  static constexpr int issued_before(int M) { return tab.before[M]; }
  static constexpr int allowed(int st) {
    int n = tab.before[bar_step(st)] - need(st);
    for (int m = 0; m < bar_step(st); ++m) if (tab.before[m + 1] >= need(st)) n += stores_at(m);
    return n;
  }
  static constexpr int xoff(int st) { return slot(PS * st); }
  static constexpr int woff(int st) { return st < 7 ? slot(PS * st + 2) : slot(BU + 2 * (st % 7)); }
  static constexpr int coff(int st) { return slot(PS * st + 4); }
};
template <int NEW, int OLD> DI unsigned rebase(unsigned& a) {
  if constexpr (NEW / K1_WIN != OLD / K1_WIN) { a += (NEW / K1_WIN - OLD / K1_WIN) * K1_WIN; PIN(a); }
  return a + NEW % K1_WIN;
}
DI f32x4 sum2(u32x4 sel, unsigned idx, bf16x8 f0, bf16x8 f1, f32x4 c) {
  typedef short s16x16 __attribute__((ext_vector_type(16)));
  const s16x16 b = __builtin_shufflevector(f0, f1, 0, 1, 2, 3, 4, 5, 6, 7, 8, 9, 10, 11, 12, 13, 14, 15);
  return __builtin_amdgcn_smfmac_f32_16x16x64_bf16(__builtin_bit_cast(bf16v8, sel), __builtin_bit_cast(bf16v16, b), c, idx, 0, 0);
}

template <bool ISV>
DI void k1_body(u32x4 xs, u32x4 wAs, u32x4 wBs, u32x4 wCs, char* ws, int rb, int cg, unsigned lds0) {
  typedef K1Plan<ISV> P;
  constexpr int pitch = ISV ? 3 * DM * 2 : FF * 2;
  static_assert(P::issued_before(P::bar_step(10)) == P::TOT && P::allowed(7) >= 0, "plan");
  STAMP_DECL; STAMP(0); STAMP(1);
  const int tid = threadIdx.x, lane = tid & 63, w = __builtin_amdgcn_readfirstlane(tid >> 6);
  const int tt = w & 3, ch = w >> 2, r = lane & 31, h = lane >> 5;
  const int xrow = 8 * w + (lane >> 3);
  const unsigned xvoff = xrow * (DM * 2) + (((lane & 7) ^ ((xrow >> 1) & 7)) << 4);
  const unsigned wvoff = ((lane >> 3) * 4 + tt) * pitch + ((lane & 7) << 4);
  const unsigned cvoff = ((lane >> 3) * 4 + tt + 32 * ch) * (3 * DM * 2) + ((lane & 7) << 4);
  auto issue = [&](auto U) {
    constexpr int u = U.value;
    constexpr unsigned slot = P::slot(u);
    if constexpr (u < P::BU) {
      constexpr int c = u / P::PS, k = u % P::PS;
      if constexpr (k < 2) dma_c<k * 64 * (DM * 2) + c * 128, slot>(xs, xvoff, lds0 + w * 1024);
      else if constexpr (k < 4) dma_c<(64 * c + 32 * (k - 2)) * pitch, slot>(wAs, wvoff, lds0 + w * 1056);
      else dma_c<64 * c * (3 * DM * 2), slot>(wCs, cvoff, lds0 + w * 1088);
    } else {
      dma_c<32 * (u - P::BU) * pitch, slot>(wBs, wvoff, lds0 + w * 1056);
    }
  };
  unroll<P::PS>([&](auto U) { issue(U); });
  asm volatile("s_barrier" ::: "memory");
  unroll<P::P0 - P::PS>([&](auto U) { issue(std::integral_constant<int, P::PS + U.value>{}); });
  const int rot = tt + 2 * ch;
  const int g = (lane >> 4) & 1, qq = (lane & 15) >> 2, p = lane & 3;
  unsigned xa[4], wa[4], ca[4];
  {
    const int row = 32 * (tt & 1) + r;
#pragma unroll
    for (int i = 0; i < 4; ++i) {
      const int s = (i + rot) & 3;
      xa[i] = lds0 + (tt >> 1) * K1_SLOT + row * 128 + (((2 * s + h) ^ ((row >> 1) & 7)) << 4);
      wa[i] = lds0 + (s >> 1) * K1_SLOT + (4 * g + qq) * 1056 + (4 * (s & 1) + 2 * h) * 128 + ch * 64 + p * 8;
      ca[i] = lds0 + (4 * (s >> 1) + qq) * 1088 + (4 * (s & 1) + 2 * h) * 128 + ch * 64 + g * 32 + p * 8;
    }
  }
  u32x4 sel2, sel1, selx;
  unsigned sidx;
  {
    const int rho = lane & 15, ga = lane >> 4;
    const bool on = rho < 8 && (ga >> 1) == (rho >> 2);
    const unsigned a0 = on && !(rho & 2) ? 0x3f803f80u : 0u, a1 = on && (rho & 2) ? 0x3f803f80u : 0u;
    sel2 = u32x4{a0, a0, a1, a1};
    sidx = (rho & 1) ? 0xEEEEEEEEu : 0x44444444u;
    const unsigned b0 = rho == (ga & 1) ? 0x3f803f80u : 0u;
    sel1 = u32x4{b0, b0, b0, b0};
    const unsigned c0 = rho < 2 ? 0x3f803f80u : 0u, c1 = (rho >> 1) == 1 ? 0x3f803f80u : 0u;
    selx = u32x4{c0, c0, c1, c1};
  }
  constexpr int LB = P::LB;
  bf16x8 xf[28];
  bf16x8 wf[LB + 1][3];
  f32x16 acc[4];
  f32x4 cs2 = {}, cs1 = {};
  f32x4 sx = {}, sxx[2] = {};
  float rden = 0.f, den = 0.f, gsc = 0.f;
  bf16x8 mfrag = {};
  u32x4 outp = {};
  const int T = 4 * rb + tt;
  const rsrc_t wr = ws_rsrc(ws);
  LAS float* csl = (LAS float*)(uintptr_t)(lds0 + K1_CS);
  LAS f32x2* stl = (LAS f32x2*)(uintptr_t)(lds0 + K1_ST);
  LAS float* stf = (LAS float*)(uintptr_t)(lds0 + K1_ST);
  const f32x16 zero = {};

  auto rowstat = [&](f32x2 a, f32x2 b, float& mean) {
    mean = (a[0] + b[0]) * (1.f / DM);
    const float var = __builtin_fmaxf((a[1] + b[1]) * (1.f / DM) - mean * mean, 0.f);
    return bfround(__builtin_amdgcn_sqrtf(bfround(bfround(var) + 1e-5f)));
  };
  auto cs_load = [&](int tile) { const LAS float* q = csl + tile * 128 + r; return f32x4{q[0], q[32], q[64], q[96]}; };
  auto cs_frag = [&](const f32x4& q) {
    const float c = (q[0] + q[1]) + (q[2] + q[3]);
    const float hi = bfround(c);
    unsigned u = cvtpk(hi, c - hi);
    PIN(u);
    return u;
  };
  auto correct = [&](const f32x16& a, unsigned u) {
    const u32x4 cf = {u, 0u, 0u, 0u};
    f32x16 o;
    if constexpr (ISV) o = MFMA(mfrag, __builtin_bit_cast(bf16x8, cf), a);
    else o = MFMA(__builtin_bit_cast(bf16x8, cf), mfrag, a);
    PIN(o);
    return o;
  };
  auto xsq = [&](const bf16x8& f, int j) {
    const s16x4 v = j ? __builtin_shufflevector(f, f, 4, 5, 6, 7) : __builtin_shufflevector(f, f, 0, 1, 2, 3);
    sxx[j] = __builtin_amdgcn_mfma_f32_4x4x4bf16_1k(v, v, sxx[j], 0, 0, 0);
    PIN(sxx[j]);
  };
  auto glu_op = [&](f32x16& a, int e, int op) {
    float t = a[e];
    if (op == 0) t = t * gsc;
    if (op == 1) t = __builtin_amdgcn_exp2f(t);
    if (op == 2) t = __builtin_fmaf(t, den, den);
    if (op == 3) t = __builtin_amdgcn_rcpf(t);
    if (op == 4) t = t * a[8 + e];
    if (op < 5) { PIN(t); a[e] = t; }
    if (op == 5 && (e & 1)) { unsigned u = cvtpk(a[e - 1], a[e]); PIN(u); outp[e >> 1] = u; }
  };
  auto store_h = [&](int half, int tl) { st16(wr, WS_H + (T * 112 + 8 * cg + 4 * half + 2 * ch + tl) * 1024 + lane * 16, outp); };
  f32x16 rs;
  auto store_v = [&](const f32x16& a, int half, int tl) {
    const int col = 256 * (cg - 14) + 128 * half + 64 * g + 16 * (2 * ch + tl) + (lane & 15);
    const int head = (col * 2341) >> 16, d = col - 28 * head;
    const f32x16 v = a * rs;
    const unsigned dst = col < DM ? WS_KV + ((head * 64 + T) * 4 + 2) * 1024 + (d + 32 * h) * 16 : OOB;
    st16(wr, dst, pack8(v, 0));
    st16(wr, dst + 1024, pack8(v, 8));
  };
  const bool isk = cg >= 7;
  auto store_pad = [&]() {
    if constexpr (ISV) {
      const int head = 8 * (cg - 14) + 4 * ch + (lane >> 4);
      const unsigned pv = (lane & 3) == 0 ? 0x3f803f80u : 0u;
      st16(wr, WS_KV + ((head * 64 + T) * 4 + 2 + ((lane >> 3) & 1)) * 1024 + (28 + (lane & 3) + 32 * ((lane >> 2) & 1)) * 16, u32x4{pv, pv, pv, pv});
    } else {
      const int head = 4 * (cg - 7) + 2 * ch + h;
      st8(wr, isk && head < NH ? WS_KV + (head * 64 + T) * 4096 + 1024 + (r + 32) * 16 + 8 : OOB, u32x2{0x3f80u, 0u});
    }
  };
  auto qk_off = [&](int Q) {
    const int head = (Q * 147) >> 10, dq = Q - 7 * head;
    return (int)(isk ? WS_KV : WS_Q) + ((head * 64 + T) << (isk ? 12 : 11)) + (dq >> 2) * 1024 + (dq & 1) * 512 + ((dq >> 1) & 1) * 8;
  };
  const int qk_lane = 512 + 16 * (isk ? ((r & 0x13) | ((r & 4) << 1) | ((r & 8) >> 1)) : r);
  int qo[4], qd[4];
#pragma unroll
  for (int a4 = 0; a4 < 4; ++a4) {
    const int Q = 16 * (isk ? cg - 7 : cg) + 8 * ch + 2 * a4;
    qo[a4] = qk_off(Q) - 512; qd[a4] = qk_off(Q + 1) - qk_off(Q);
    if constexpr (!ISV) asm volatile("" : "+s"(qo[a4]), "+s"(qd[a4]));
  }
  u32x2 qkv;
  auto qk_part = [&](const f32x16& a, int a4, int part) {
    const float sc = isk ? rden : rden * QSCALE;
    unsigned u = cvtpk(a[4 * a4 + 2 * part] * sc, a[4 * a4 + 2 * part + 1] * sc);
    PIN(u);
    qkv[part] = u;
    if (part == 1) {
      st8(wr, qk_lane + (h ? qd[a4] : 0), qkv, qo[a4]);
    }
  };
  auto sync = [&](auto ST) {
    constexpr int st = ST.value;
    if constexpr (P::need(st) > 0) wait_vm_bar<P::allowed(st)>(); else wait_lds_bar();
    STAMP(2 + st);
  };
  auto reads = [&](auto GG, auto PART) {
    constexpr int G = GG.value, st = G >> 2, i = G & 3, b = G % (LB + 1), ps = st > 0 ? st - 1 : 0, part = PART.value;
    if constexpr (part == 0) {
      const unsigned aw = rebase<P::woff(st), (st > 0 ? P::woff(ps) : 0)>(wa[i]);
      if constexpr (st < 7) xf[G] = ldsfrag(rebase<P::xoff(st), (st > 0 ? P::xoff(ps) : 0)>(xa[i]));
      if constexpr (st < 14) wf[b][0] = trfrag(aw, aw + 128); else wf[b][0] = trfrag(aw + 32, aw + 160);
    } else {
      const unsigned aw = wa[i] + P::woff(st) % K1_WIN;
      if constexpr (st < 7 || (st < 14 && i == 0)) wf[b][1] = trfrag(aw + 32, aw + 160);
      if constexpr (st < 7 && !ISV) { const unsigned ac = rebase<P::coff(st), (st > 0 ? P::coff(ps) : 0)>(ca[i]); wf[b][2] = trfrag(ac, ac + 128); }
    }
    SBAR();
  };
  auto mm = [&](f32x16& a, const f32x16& cin, const bf16x8& wfr, const bf16x8& xfr) {
    if constexpr (ISV) a = MFMA(xfr, wfr, cin); else a = MFMA(wfr, xfr, cin);
    PIN(a); SBAR();
  };
#define IC(n) std::integral_constant<int, (n)>{}
  f32x2 st0, st1;
  f32x4 cq[3];
  unsigned cu[3];
  sync(IC(0)); reads(IC(0), IC(0)); reads(IC(0), IC(1));
  unroll<P::NG>([&](auto MM) {
    constexpr int M = MM.value, pass = M / 28, ks = M % 28, b = M % (LB + 1), pos = M & 3;
    constexpr int n0 = P::issued_before(M), n1 = P::issued_before(M + 1);
    if constexpr (pass == 0) {
      constexpr int k0 = ks - pos;
      if constexpr (pos == 3) sync(IC((M + 1) >> 2));
      __builtin_amdgcn_s_setprio(3 - ((pos + 1) & 3));
      mm(acc[0], ks == 0 ? zero : acc[0], wf[b][0], xf[ks]);
      reads(IC(M + 1), IC(0));
      if constexpr (M == 27) { st0 = stl[32 * tt + r]; st1 = stl[128 + 32 * tt + r]; }
      SBAR();
      mm(acc[1], ks == 0 ? zero : acc[1], wf[b][1], xf[ks]);
      reads(IC(M + 1), IC(1));
      if constexpr (M == 27) { reads(IC(29), IC(0)); reads(IC(30), IC(0)); }
      if constexpr (n1 > n0 + 1) issue(IC(n0));
      SBAR();
      if constexpr (!ISV) mm(acc[2], ks == 0 ? zero : acc[2], wf[b][2], xf[ks]);
      if constexpr (n1 > n0) issue(IC(n1 > n0 ? n1 - 1 : 0));
      if constexpr (pos == 0) {
        cs2 = sum2(sel2, sidx, wf[b][0], wf[b][1], cs2); PIN(cs2);
        if constexpr (!ISV) { cs1 = __builtin_amdgcn_mfma_f32_16x16x32_bf16(__builtin_bit_cast(bf16x8, sel1), wf[b][2], cs1, 0, 0, 0); PIN(cs1); }
      }
      if constexpr (pos == 1) { xsq(xf[k0], 0); xsq(xf[k0], 1); sx = sum2(selx, sidx, xf[k0], xf[k0 + 1], sx); PIN(sx); }
      if constexpr (pos == (M < 24 ? 2 : 1)) { xsq(xf[k0 + 1], 0); xsq(xf[k0 + 1], 1); }
      if constexpr (M == 25) {
        if (lane < 32) {
          LAS float* q = csl + (2 * ch + g) * 128 + tt * 32 + (lane & 15);
          q[0] = cs2[0] + cs2[1]; q[16] = cs2[2] + cs2[3];
          if (!ISV && lane < 16) { LAS float* q1 = csl + (8 + ch) * 128 + tt * 32 + lane; q1[0] = cs1[0]; q1[16] = cs1[1]; }
        }
        cs2 = f32x4{};
      }
      if constexpr (M == 26) {
        const f32x4 q4 = sxx[0] + sxx[1];
        const float q2 = swap32_add((lane & 2) ? ((lane & 1) ? q4[3] : q4[2]) : ((lane & 1) ? q4[1] : q4[0]));
        if (h == 0) {
          stf[2 * (128 * ch + 32 * tt + r) + 1] = q2;
          if (lane < 16) { stf[2 * (128 * ch + 32 * tt + lane)] = sx[0] + sx[1]; stf[2 * (128 * ch + 32 * tt + 16 + lane)] = sx[2] + sx[3]; }
        }
      }
    } else {
      f32x16& a = acc[pass == 1 ? 3 : 2];
      if constexpr (M + LB < P::NG && ((M + LB) & 3) == 0 && P::need((M + LB) >> 2) >= 0) sync(IC((M + LB) >> 2));
      if constexpr (M == 30 || M == 41 || M == 61) __builtin_amdgcn_s_setprio(2);
      if constexpr (M == 32 || M == 45 || M == 69) __builtin_amdgcn_s_setprio(1);
      if constexpr (M == 35 || M == 49 || M == 77) __builtin_amdgcn_s_setprio(0);
      if constexpr (M == 37 || M == 53) __builtin_amdgcn_s_setprio(3);
      if constexpr (M == 56) a = correct(zero, cu[1]);
      mm(a, M == 28 ? zero : a, wf[b][0], xf[ks]);
      if constexpr (M + LB < P::NG) { reads(IC(M + LB < P::NG ? M + LB : 0), IC(0)); reads(IC(M + LB < P::NG ? M + LB : 0), IC(1)); }
      if constexpr (n1 > n0) issue(IC(n0));
      if constexpr (n1 > n0 + 1) issue(IC(n0 + 1));
      if constexpr (pass == 1 && pos == 0) { cs2 = sum2(sel2, sidx, wf[b][0], wf[b][1], cs2); PIN(cs2); }
      if constexpr (M == 28) {
        if constexpr (!ISV) cq[2] = cs_load(8 + ch);
        cq[0] = cs_load(2 * ch); cq[1] = cs_load(2 * ch + 1);
        float mean;
        den = rowstat(st0, st1, mean);
        const u32x4 mf = {h ? 0u : cvtpk(-mean, -mean), 0u, 0u, 0u};
        mfrag = __builtin_bit_cast(bf16x8, mf);
        PIN(mfrag); PIN(den);
      }
      if constexpr (M == 29) { rden = __builtin_amdgcn_rcpf(den); gsc = -LOG2E * rden; PIN(rden); PIN(gsc); store_pad(); }
      if constexpr (M == 30) { if constexpr (!ISV) cu[2] = cs_frag(cq[2]); cu[0] = cs_frag(cq[0]); }
      if constexpr (M == 31) { if constexpr (!ISV) acc[2] = correct(acc[2], cu[2]); cu[1] = cs_frag(cq[1]); }
      if constexpr (M == 32) acc[0] = correct(acc[0], cu[0]);
      if constexpr (M == 33) acc[1] = correct(acc[1], cu[1]);
      if constexpr (M == 53) { cq[0] = cs_load(4 + 2 * ch); cq[1] = cs_load(4 + 2 * ch + 1); }
      if constexpr (M == 55) { cu[0] = cs_frag(cq[0]); cu[1] = cs_frag(cq[1]); }
      if constexpr (M == 57) acc[3] = correct(acc[3], cu[0]);
      if constexpr (ISV) {
        if constexpr (M >= 32 && M < 40) {
          constexpr int e = 2 * (M - 32);
#pragma unroll
          for (int j = e; j < e + 2; ++j) { const int t = 32 * tt + (j & 3) + 8 * (j >> 2) + 4 * h; float mean; rs[j] = __builtin_amdgcn_rcpf(rowstat(stl[t], stl[128 + t], mean)); }
          PIN(rs);
        }
        if constexpr (M == 28 + 14) store_v(acc[0], 0, 0);
        if constexpr (M == 28 + 20) store_v(acc[1], 0, 1);
        if constexpr (M == 56 + 6) store_v(acc[3], 1, 0);
      } else {
        if constexpr (M >= 34 && M < 42) qk_part(acc[2], (M - 34) >> 1, (M - 34) & 1);
        unroll<24>([&](auto NN) {
          constexpr int n = NN.value, op = M - (42 + (3 * n) / 2);
          if constexpr (op >= 0 && op < 6) {
            glu_op(acc[n < 8 ? 0 : n < 16 ? 1 : 3], n & 7, op);
            if constexpr (op == 5 && (n & 7) == 7) store_h(n >> 4, (n >> 3) & 1);
          }
        });
      }
      if constexpr (M == 28 + 24) {
        if (lane < 32) {
          LAS float* q = csl + (4 + 2 * ch + g) * 128 + tt * 32 + (lane & 15);
          q[0] = cs2[0] + cs2[1]; q[16] = cs2[2] + cs2[3];
        }
      }
    }
    SBAR();
  });
  STAMP(24);
  if constexpr (ISV) store_v(acc[2], 1, 1);
  else {
#pragma unroll
    for (int op = 0; op < 6; ++op)
#pragma unroll
      for (int e = 0; e < 8; ++e) glu_op(acc[2], e, op);
    store_h(1, 1);
  }
  STAMP(25);
  STAMP_FLUSH(ws, 0);
}

__global__ __launch_bounds__(512, 2) void k1_proj(const char* x, const char* wqkv, const char* wg, const char* wu, char* ws) {
  extern __shared__ __attribute__((aligned(16))) char smem[];
  const unsigned lds0 = (unsigned)(uintptr_t)smem;
  const int xcd = blockIdx.x & 7, idx = blockIdx.x >> 3;
  const int rb = (xcd & 1) * 8 + (idx & 7), cg = (xcd >> 1) * 4 + (idx >> 3);
  const int b = __builtin_amdgcn_readfirstlane(threadIdx.x >> 8);
  const u32x4 xs = make_srd(x + (size_t)rb * 128 * (DM * 2), 128 * DM * 2);
  if (cg < 14) {
    const char* wm = (b ? wu : wg) + cg * 256;
    k1_body<false>(xs, make_srd(wm, DM * FF * 2 - cg * 256), make_srd(wm + 128, DM * FF * 2 - cg * 256 - 128),
                   make_srd(wqkv + cg * 128, DM * 3 * DM * 2 - cg * 128), ws, rb, cg, lds0);
  } else {
    const int oa = 2 * DM * 2 + (cg - 14) * 512 + b * 128, ob = 2 * DM * 2 + (cg - 14) * 512 + 256 + (cg == 14 ? b * 128 : 0);
    k1_body<true>(xs, make_srd(wqkv + oa, DM * 3 * DM * 2 - oa), make_srd(wqkv + ob, DM * 3 * DM * 2 - ob), xs, ws, rb, cg, lds0);
  }
}

constexpr int K2_LDS = 163840;
DI void put_tile(unsigned a, const f32x16& v) {
#pragma unroll
  for (int j = 0; j < 4; ++j) { const f32x4 t = {v[4 * j], v[4 * j + 1], v[4 * j + 2], v[4 * j + 3]}; *(LAS f32x4*)(uintptr_t)(a + j * 1024) = t; }
}
DI void add_tile(unsigned a, f32x16& v) {
#pragma unroll
  for (int j = 0; j < 4; ++j) { const f32x4 t = *(const LAS f32x4*)(uintptr_t)(a + j * 1024);
#pragma unroll
    for (int e = 0; e < 4; ++e) v[4 * j + e] += t[e]; }
}
DI float rowmax16(const f32x16& s) {
  float a = __builtin_fmaxf(__builtin_fmaxf(s[0], s[1]), s[2]), b = __builtin_fmaxf(__builtin_fmaxf(s[3], s[4]), s[5]);
  a = __builtin_fmaxf(__builtin_fmaxf(a, s[6]), s[7]); b = __builtin_fmaxf(__builtin_fmaxf(b, s[8]), s[9]);
  a = __builtin_fmaxf(__builtin_fmaxf(a, s[10]), s[11]); b = __builtin_fmaxf(__builtin_fmaxf(b, s[12]), s[13]);
  a = __builtin_fmaxf(__builtin_fmaxf(a, s[14]), s[15]);
  return swap32_max(__builtin_fmaxf(a, b));
}
constexpr int quad_fs(int q) { return q < 14 ? 8 * q : 8 * (q - 14) + 4; }

__global__ __launch_bounds__(512, 2) void k2_attn_down(const char* wd, char* ws) {
  extern __shared__ __attribute__((aligned(16))) char smem[];
  const unsigned lds0 = (unsigned)(uintptr_t)smem;
  const int xcd = blockIdx.x & 7, idx = blockIdx.x >> 3;
  const int w = __builtin_amdgcn_readfirstlane(threadIdx.x >> 6);
  const int head = xcd * 2 + (idx >> 4), qb = idx & 15;
  const bool live = idx < 28;
  const int gi = live ? idx : idx - 28, tp = xcd * 4 + gi / 7, np = gi % 7;
  const int lane = threadIdx.x & 63, r = lane & 31, h = lane >> 5, qt = w & 3, kh = w >> 2;
  STAMP_DECL; STAMP(0); STAMP(1);
  const u32x4 wss = make_srd(ws, 32u << 20), wds = make_srd(wd + np * 128, FF * DM * 2 - np * 128);
  const bf16x8* qp = (const bf16x8*)(ws + WS_Q + (size_t)(head * 64 + 4 * qb + qt) * 2048) + lane;
  bf16x8 q0, q1;
  asm volatile("global_load_dwordx4 %0, %2, off\n\tglobal_load_dwordx4 %1, %2, off offset:1024" : "=&v"(q0), "=&v"(q1) : "v"(qp) : "memory");
  const unsigned l16 = lane * 16;
  const unsigned kvb = WS_KV + head * 64 * 4096;
  set_m0(lds0 + 65536 + w * 2048);
  { const unsigned so = kvb + kh * 32 * 4096 + (qt < 3 ? qt * 4096 : 2048); dma_m0<0>(wss, l16, so); dma_m0<1024>(wss, l16, so); }
  const int wrow = lane >> 3;
  const unsigned wvoff = wrow * (DM * 2) + ((((lane >> 2) & 1) ^ ((wrow >> 1) & 1)) << 6) + ((lane & 3) << 4);
  const bool kvw = w < 4;
  const u32x4 sA = kvw ? wss : wds;
  const unsigned vA0 = kvw ? l16 + 3072 : wvoff, vA1 = kvw ? l16 + 4096 + 2048 - 1024 : wvoff + 8 * (DM * 2) - 1024;
  const unsigned vB1 = kvw ? l16 : l16 + 111 * 1024;
  const unsigned hb = WS_H + 2 * tp * 112 * 1024 - 2048;
  unsigned oA = kvw ? kvb + ((w >> 1) * 32 + (w & 1)) * 4096 : (w - 4) * 16 * (DM * 2);
  unsigned oB = kvw ? oA + 3 * 4096 - 2048 : hb + (w - 4) * 1024;
  const unsigned dA = kvw ? 8192 : 8 * 16 * (DM * 2), dB = 8192;
  const unsigned jA = kvw ? 8192 : (unsigned)((4 - 104) * 16 * (DM * 2)), jB = kvw ? 8192 : (unsigned)((4 - 104) * 1024);
  const unsigned mw = lds0 + w * 4096;
#define LD0(slot) do { set_m0(mw + (slot)); dma_m0<0>(sA, vA0, oA); } while (0)
#define LD1() dma_m0<1024>(sA, vA1, oA)
#define LD2() dma_m0<2048>(wss, l16, oB)
#define LD3(jump) do { dma_m0<3072>(wss, vB1, oB); oA += (jump) ? jA : dA; oB += (jump) ? jB : dB; } while (0)
  LD0(0); LD1(); LD2(); LD3(false);
  const f32x16 zero = {};
  unsigned pidx = 0xDDDDDDDDu;
  f32x16 o = zero, ga = zero, s[4];
  s[3] = zero;
  bf16x8 k0, k1, v0, v1 = {}, gw = {}, gh = {};
  const int g = (lane >> 4) & 1, qq = (lane & 15) >> 2, p = lane & 3;
  const unsigned gl = (4 * h + qq) * 128 + ((((qq >> 1) & 1) ^ (qt & 1)) << 6) + g * 32 + p * 8;
  unsigned ak = lds0 + kh * 8192 + l16;
  unsigned aw = lds0 + 16384 + kh * 8192 + gl, ah = lds0 + 16384 + kh * 8192 + 2048 + (qt >> 1) * 1024 + l16;
  PIN(pidx); PIN(o); PIN(ga); PIN(s[3]); PIN(v1); PIN(gw); PIN(gh);
  asm volatile("s_waitcnt vmcnt(4)\n\ts_barrier" : "+v"(q0), "+v"(q1)::"memory");
  LD0(32768); LD1(); LD2(); LD3(false);
  {
    const unsigned st = lds0 + 65536 + l16;
    u32x4 qv = __builtin_bit_cast(u32x4, q1);
    if (h) { qv[2] = 0u; qv[3] = 0u; }
    f32x16 t = MFMA(ldsfrag(st), q0, zero);
    t = MFMA(ldsfrag(st + 1024), __builtin_bit_cast(bf16x8, qv), t);
    const unsigned so = st + kh * 8192;
    s[0] = MFMA(ldsfrag(so), q0, zero);
    s[1] = MFMA(ldsfrag(so + 2048), q0, zero);
    const float m = rowmax16(t);
    if (h) qv[2] = cvtpk(-m, 0.f);
    q1 = __builtin_bit_cast(bf16x8, qv);
    s[0] = MFMA(ldsfrag(so + 1024), q1, s[0]);
    s[1] = MFMA(ldsfrag(so + 3072), q1, s[1]);
    k0 = ldsfrag(so + 4096); k1 = ldsfrag(so + 5120); v0 = ldsfrag(so + 6144);
    PIN(k0); PIN(k1); PIN(v0); PIN(s[0]); PIN(s[1]);
    asm volatile("s_nop 7\n\ts_nop 7" ::: "memory");
  }
  STAMP(2);
#define EXN(S, i, n) do { _Pragma("unroll") for (int e_ = (i); e_ < (i) + (n); ++e_) { float t_ = __builtin_amdgcn_exp2f(S[e_]); PIN(t_); S[e_] = t_; } SBAR(); } while (0)
#define PRIO(n) __builtin_amdgcn_s_setprio(n)
#define ASTEP(J, O, D0, D1) do { constexpr int JP = (J + 3) & 3, JN = (J + 2) & 3; \
    PRIO(3 - 2 * (J & 1)); \
    EXN(s[J], 0, 3);  o = pv_mfma<8>(v1, s[JP], o, pidx); PIN(o); SBAR(); v1 = ldsfrag(ak + (O)); SBAR(); \
    EXN(s[J], 3, 3);  s[JN] = MFMA(k0, q0, zero); PIN(s[JN]); SBAR(); k0 = ldsfrag(ak + (O) + 2048); D0; SBAR(); \
    EXN(s[J], 6, 4);  o = pv_mfma<0>(v0, s[J], o, pidx); PIN(o); SBAR(); v0 = ldsfrag(ak + (O) + 1024); PRIO(2 - 2 * (J & 1)); SBAR(); \
    EXN(s[J], 10, 3); s[JN] = MFMA(k1, q1, s[JN]); PIN(s[JN]); SBAR(); k1 = ldsfrag(ak + (O) + 3072); D1; SBAR(); \
    EXN(s[J], 13, 3); ga = MFMA(gw, gh, ga); PIN(ga); SBAR(); \
    gw = trfrag(aw + (O), aw + (O) + 1024); gh = ldsfrag(ah + (O)); SBAR(); } while (0)
  unsigned wsl = 65536;
#pragma unroll 1
  for (int t = 0; t < 16; t += 2) {
    wait_vm_rawbar<4>();
    ASTEP(0, 0, LD0(wsl), LD1());
    ASTEP(1, 4096, LD2(), LD3(false));
    wait_vm_rawbar<4>();
    ASTEP(2, 32768, LD0(wsl + 32768), LD1());
    ASTEP(3, 32768 + 4096, LD2(), LD3(t == 10));
    wsl ^= 65536; ak ^= 65536; aw ^= 65536; ah ^= 65536;
  }
  o = pv_mfma<8>(v1, s[3], o, pidx);
  ga = MFMA(gw, gh, ga);
#undef ldsfrag
#undef trfrag
#undef ASTEP
  STAMP(3);
  const unsigned tA = qt * 16 * (DM * 2), tB = hb + qt * 1024;
#define TLD(quad, d) do { set_m0(lds0 + (d) + qt * 4096); const unsigned a_ = tA + quad_fs(14) * 16 * (DM * 2) + ((quad) - 14) * 8 * 16 * (DM * 2), \
    b_ = tB + quad_fs(14) * 1024 + ((quad) - 14) * 8192; \
    dma_m0<0>(wds, wvoff, a_); dma_m0<1024>(wds, wvoff2, a_); dma_m0<2048>(wss, l16, b_); dma_m0<3072>(wss, l16b, b_); } while (0)
#define TMM(d) do { _Pragma("unroll") for (int j_ = 0; j_ < 2; ++j_) { const unsigned x_ = lds0 + (d) + kh * 8192 + j_ * 4096; \
    ga = MFMA(trfrag(x_ + gl, x_ + gl + 1024), ldsfrag(x_ + 2048 + (qt >> 1) * 1024 + l16), ga); } } while (0)
  const unsigned wvoff2 = wvoff + 8 * (DM * 2) - 1024, l16b = l16 + 111 * 1024;
  TLD(20 + kh, 131072 + kh * 16384);
  if (kh) put_tile(lds0 + 65536 + qt * 4096 + l16, o);
  wait_lds_bar();
  TLD(18 + kh, 65536 + 16384 + kh * 32768);
  STAMP(4);
  if (!kh) {
    add_tile(lds0 + 65536 + qt * 4096 + l16, o);
    float l = upper_half(o[12]);
    float rl = __builtin_amdgcn_rcpf(l);
    bool bad = !(l < 3.0e38f);
#pragma unroll
    for (int i = 0; i < 16; ++i) { o[i] *= rl; bad |= !(__builtin_fabsf(o[i]) < 3.0e38f); }
    if (__builtin_amdgcn_ballot_w64(bad) != 0) {
      const bf16x8* kv = (const bf16x8*)(ws + kvb) + lane;
      u32x4 qv = __builtin_bit_cast(u32x4, q1);
      if (h) qv[2] = 0u;
      q1 = __builtin_bit_cast(bf16x8, qv);
      float m = -3.0e38f;
      o = zero;
#pragma unroll 1
      for (int t = 0; t < 64; ++t) {
        k0 = kv[t * 256]; k1 = kv[t * 256 + 64]; v0 = kv[t * 256 + 128]; v1 = kv[t * 256 + 192];
        f32x16 u = MFMA(k0, q0, zero);
        u = MFMA(k1, q1, u);
        const float mn = __builtin_fmaxf(m, rowmax16(u));
        const float al = __builtin_amdgcn_exp2f(m - mn);
        m = mn;
#pragma unroll
        for (int i = 0; i < 16; ++i) { o[i] *= al; u[i] = __builtin_amdgcn_exp2f(u[i] - mn); }
        o = pv_mfma<0>(v0, u, o, pidx);
        o = pv_mfma<8>(v1, u, o, pidx);
      }
      l = upper_half(o[12]);
      rl = __builtin_amdgcn_rcpf(l);
#pragma unroll
      for (int i = 0; i < 16; ++i) o[i] *= rl;
    }
    const rsrc_t wr = ws_rsrc(ws);
    const unsigned ob = WS_O + (4 * qb + qt) * 28 * 1024;
#pragma unroll
    for (int a4 = 0; a4 < 4; ++a4) {
      const int dq = 2 * a4 + h, Q = 7 * head + dq;
      const u32x2 v = {cvtpk(o[4 * a4], o[4 * a4 + 1]), cvtpk(o[4 * a4 + 2], o[4 * a4 + 3])};
      st8(wr, dq < 7 ? ob + (Q >> 2) * 1024 + (r + 32 * (Q & 1)) * 16 + ((Q >> 1) & 1) * 8 : OOB, v);
    }
    wait_vm_bar<4>();
  } else {
    wait_vm_bar<0>();
  }
  STAMP(5);
  TLD(22 + kh, kh * 32768);
  TMM(16384); TMM(32768 + 16384);
  TLD(24 + kh, 65536 + kh * 32768);
  TMM(65536 + 16384); TMM(98304 + 16384);
  wait_lds_bar();
  TLD(26 + kh, 16384 + kh * 32768);
  TMM(131072); TMM(131072 + 16384);
  wait_vm_bar<8>();
  TMM(0); TMM(32768);
  wait_vm_bar<4>();
  TMM(65536); TMM(98304);
  wait_vm_bar<0>();
  TMM(16384); TMM(32768 + 16384);
  STAMP(6);
  if (!kh) put_tile(lds0 + 131072 + qt * 4096 + l16, ga);
  wait_lds_bar();
  if (kh && live) {
    add_tile(lds0 + 131072 + qt * 4096 + l16, ga);
#pragma unroll
    for (int i = 0; i < 16; ++i) ga[i] = ga[i] * __builtin_amdgcn_rcpf(1.f + __builtin_amdgcn_exp2f(-LOG2E * ga[i]));
    const rsrc_t wr = ws_rsrc(ws);
    const unsigned dst = WS_G + ((2 * tp + (qt >> 1)) * 14 + 2 * np + (qt & 1)) * 2048 + l16;
    st16(wr, dst, pack8(ga, 0));
    st16(wr, dst + 1024, pack8(ga, 8));
  }
  STAMP(7);
  STAMP_FLUSH(ws, 1);
}

constexpr int K3_LDS = 28 * 4096;
__global__ __launch_bounds__(256, 1) void k3_out(const char* x, const char* wo, const char* ws, char* out) {
  extern __shared__ __attribute__((aligned(16))) char smem[];
  const unsigned lds0 = (unsigned)(uintptr_t)smem;
  const int xcd = blockIdx.x & 7, idx = blockIdx.x >> 3;
  const int tp = xcd * 4 + idx / 7, np = idx % 7;
  const int lane = threadIdx.x & 63, w = __builtin_amdgcn_readfirstlane(threadIdx.x >> 6), r = lane & 31, h = lane >> 5;
  STAMP_DECL; STAMP(0); STAMP(1);
  const int wr = lane >> 3;
  const unsigned wvoff = wr * (DM * 2) + ((((lane >> 2) & 1) ^ ((wr >> 1) & 1)) << 6) + ((lane & 3) << 4);
  const unsigned voff = w < 2 ? wvoff : lane * 16;
  const char* src = w < 2 ? wo + np * 128 + w * 8 * (DM * 2) : ws + WS_O + (size_t)(2 * tp + (w - 2)) * 28 * 1024;
  const int step = w < 2 ? 16 * DM * 2 : 1024;
#pragma unroll
  for (int ks = 0; ks < 28; ++ks) glds16(src + ks * step, voff, lds0 + ks * 4096 + w * 1024);
  const int T = 2 * tp + (w >> 1), NT = 2 * np + (w & 1);
  const size_t xo = (size_t)(32 * T + r) * (DM * 2) + NT * 64 + h * 8;
  u32x2 xv[4]; u32x4 gv[2];
  asm volatile("global_load_dwordx2 %0, %4, off\n\tglobal_load_dwordx2 %1, %4, off offset:16\n\tglobal_load_dwordx2 %2, %4, off offset:32\n\tglobal_load_dwordx2 %3, %4, off offset:48"
               : "=&v"(xv[0]), "=&v"(xv[1]), "=&v"(xv[2]), "=&v"(xv[3]) : "v"(x + xo) : "memory");
  asm volatile("global_load_dwordx4 %0, %2, off\n\tglobal_load_dwordx4 %1, %2, off offset:1024"
               : "=&v"(gv[0]), "=&v"(gv[1]) : "v"(ws + WS_G + (size_t)(T * 14 + NT) * 2048 + lane * 16) : "memory");
  const int g = (lane >> 4) & 1, qq = (lane & 15) >> 2, p = lane & 3;
  const unsigned wa = lds0 + (4 * h + qq) * 128 + ((((qq >> 1) & 1) ^ (w & 1)) << 6) + g * 32 + p * 8;
  const unsigned ha = lds0 + 2048 + (w >> 1) * 1024 + lane * 16;
  f32x16 acc = {};
  unroll<4>([&](auto C) {
    constexpr int c = C.value;
    wait_vm_bar<6 + 21 - 7 * c>();
    STAMP(2 + c);
#pragma unroll
    for (int k = 0; k < 7; ++k) {
      const unsigned so = (7 * c + k) * 4096;
      acc = MFMA(trfrag(wa + so, wa + so + 1024), ldsfrag(ha + so), acc);
    }
  });
  asm volatile("s_waitcnt vmcnt(0)" : "+v"(xv[0]), "+v"(xv[1]), "+v"(xv[2]), "+v"(xv[3]), "+v"(gv[0]), "+v"(gv[1])::"memory");
#pragma unroll
  for (int a4 = 0; a4 < 4; ++a4) {
    float y[4];
#pragma unroll
    for (int j = 0; j < 4; ++j) {
      const unsigned xw = xv[a4][j >> 1], gw = gv[a4 >> 1][2 * (a4 & 1) + (j >> 1)];
      const float xf = asf((j & 1) ? (xw & 0xffff0000u) : (xw << 16));
      const float gf = asf((j & 1) ? (gw & 0xffff0000u) : (gw << 16));
      y[j] = xf + acc[4 * a4 + j] * gf;
    }
    const u32x2 v = {cvtpk(y[0], y[1]), cvtpk(y[2], y[3])};
    *(u32x2*)(out + xo + a4 * 16) = v;
  }
  STAMP(6);
  STAMP_FLUSH(ws, 2);
}

#if defined(__HIP_DEVICE_COMPILE__)
#pragma clang attribute pop
#endif

extern "C" void kernel_launch(void* const* d_in, const int* in_sizes, int n_in, void* d_out, int out_size, void* d_ws, size_t ws_size,
                              hipStream_t stream) {
  const char* x = (const char*)d_in[0];
  const char* wqkv = (const char*)d_in[1];
  const char* wo = (const char*)d_in[2];
  const char* wg = (const char*)d_in[4];
  const char* wu = (const char*)d_in[5];
  const char* wd = (const char*)d_in[6];
  char* ws = (char*)d_ws;
  (void)hipFuncSetAttribute((const void*)k1_proj, hipFuncAttributeMaxDynamicSharedMemorySize, K1_LDS);
  (void)hipFuncSetAttribute((const void*)k2_attn_down, hipFuncAttributeMaxDynamicSharedMemorySize, K2_LDS);
  (void)hipFuncSetAttribute((const void*)k3_out, hipFuncAttributeMaxDynamicSharedMemorySize, K3_LDS);
  hipLaunchKernelGGL(k1_proj, dim3(256), dim3(512), K1_LDS, stream, x, wqkv, wg, wu, ws);
  hipLaunchKernelGGL(k2_attn_down, dim3(256), dim3(512), K2_LDS, stream, wd, ws);
  hipLaunchKernelGGL(k3_out, dim3(224), dim3(256), K3_LDS, stream, x, wo, (const char*)ws, (char*)d_out);
}
```

```cpp
#include <hip/hip_runtime.h>
#include <hip/hip_bf16.h>
#include <stdint.h>
#include <utility>

#if defined(__HIP_DEVICE_COMPILE__)
#pragma clang attribute push(__attribute__((target("no-packed-fp32-ops"))), apply_to = function)
#endif
typedef short bf16x8 __attribute__((ext_vector_type(8)));
typedef short s16x4 __attribute__((ext_vector_type(4)));
typedef __bf16 bf16x2 __attribute__((ext_vector_type(2)));
typedef float f32x2 __attribute__((ext_vector_type(2)));
typedef float f32x4 __attribute__((ext_vector_type(4)));
typedef float f32x16 __attribute__((ext_vector_type(16)));
typedef unsigned u32x2 __attribute__((ext_vector_type(2)));
typedef unsigned u32x4 __attribute__((ext_vector_type(4)));
#define LAS __attribute__((address_space(3)))
#define DI __device__ __forceinline__
#define TIDX __builtin_amdgcn_workitem_id_x()
#define BIDX __builtin_amdgcn_workgroup_id_x()
#define MFMA(a, b, c) __builtin_amdgcn_mfma_f32_32x32x16_bf16((a), (b), (c), 0, 0, 0)
#define SBAR() __builtin_amdgcn_sched_barrier(0)
#define PIN(x) asm volatile("" : "+v"(x))

constexpr int SEQ = 2048, DM = 448, FF = 1792;
constexpr unsigned WS_KV = 2u << 20;
constexpr unsigned WS_H = 6u << 20;
constexpr unsigned WS_O = 14u << 20;
constexpr unsigned WS_Q = 0;
constexpr unsigned WS_F = 13u << 20;
constexpr unsigned WS_F1 = WS_F + 32768, WS_FA = WS_F + 2 * 32768, WS_F2 = WS_F + 3 * 32768, WS_F3 = WS_F + 4 * 32768;
constexpr unsigned WS_ST = WS_F + 16384;
constexpr unsigned WS_BYTES = 16u << 20;
constexpr float LOG2E = 1.4426950408889634f;
constexpr float QSCALE = 0.18898223650461362f * LOG2E;

DI float asf(unsigned u) { return __builtin_bit_cast(float, u); }
DI unsigned asu(float f) { return __builtin_bit_cast(unsigned, f); }
DI unsigned mul24(unsigned a, unsigned b) { return (a & 0xffffffu) * (b & 0xffffffu); }
DI unsigned cvtpk(float a, float b) { f32x2 v = {a, b}; bf16x2 r = __builtin_convertvector(v, bf16x2); return __builtin_bit_cast(unsigned, r); }
DI float bfround(float a) { return asf(cvtpk(a, 0.f) << 16); }
DI float swap32_max(float v) { auto r = __builtin_amdgcn_permlane32_swap(asu(v), asu(v), false, false); return __builtin_fmaxf(asf(r[0]), asf(r[1])); }
DI float upper_half(float v) { auto r = __builtin_amdgcn_permlane32_swap(asu(v), asu(v), false, false); return asf(r[1]); }
DI u32x4 make_srd(const void* p, unsigned bytes) { return u32x4{(unsigned)(uintptr_t)p, (unsigned)((uintptr_t)p >> 32) & 0xffffu, bytes, 0x00020000u}; }
template <int SOFF, int LOFF> DI void dma_c(u32x4 srd, unsigned voff, unsigned lds) {
  unsigned tmp;
  asm volatile("s_add_u32 m0, %3, %5\n\ts_mov_b32 %0, %4\n\tbuffer_load_dwordx4 %1, %2, %0 offen lds"
               : "=&s"(tmp) : "v"(voff), "s"(srd), "s"(lds), "n"(SOFF), "n"(LOFF) : "memory", "scc");
}
DI void set_m0(unsigned lds) { asm volatile("s_mov_b32 m0, %0\n\ts_nop 0" ::"s"(lds) : "memory"); }
template <int OFF> DI void dma_m0(u32x4 srd, unsigned voff, unsigned soff) {
  asm volatile("buffer_load_dwordx4 %0, %1, %2 offen offset:%3 sc1 lds" ::"v"(voff), "s"(srd), "s"(soff), "n"(OFF) : "memory");
}
typedef short v4i16_t __attribute__((ext_vector_type(4)));
DI s16x4 trread(unsigned a) { return __builtin_bit_cast(s16x4, __builtin_amdgcn_ds_read_tr16_b64_v4i16((LAS v4i16_t*)(uintptr_t)a)); }
DI bf16x8 trfrag(unsigned a0, unsigned a1) { s16x4 lo = trread(a0), hi = trread(a1); return __builtin_shufflevector(lo, hi, 0, 1, 2, 3, 4, 5, 6, 7); }
DI bf16x8 ldsfrag(unsigned a) { return *(const LAS bf16x8*)(uintptr_t)a; }
template <int N> DI void wait_vm() { asm volatile("s_waitcnt vmcnt(%0)" ::"n"(N) : "memory"); }
template <int N> DI void wait_vm_bar() { asm volatile("s_waitcnt vmcnt(%0) lgkmcnt(0)\n\ts_barrier" ::"n"(N) : "memory"); }
template <int N> DI void wait_vm_rawbar() { asm volatile("s_waitcnt vmcnt(%0)\n\ts_barrier" ::"n"(N) : "memory"); }
DI void wait_lds_bar() { asm volatile("s_waitcnt lgkmcnt(0)\n\ts_barrier" ::: "memory"); }
template <class F, int... I> DI void unroll_impl(F&& f, std::integer_sequence<int, I...>) { (f(std::integral_constant<int, I>{}), ...); }
template <int N, class F> DI void unroll(F&& f) { unroll_impl(f, std::make_integer_sequence<int, N>{}); }
DI bf16x8 pack8(const f32x16& v, int o) {
  u32x4 p = {cvtpk(v[o], v[o + 1]), cvtpk(v[o + 2], v[o + 3]), cvtpk(v[o + 4], v[o + 5]), cvtpk(v[o + 6], v[o + 7])};
  return __builtin_bit_cast(bf16x8, p);
}

typedef __bf16 bf16v8 __attribute__((ext_vector_type(8)));
typedef __bf16 bf16v16 __attribute__((ext_vector_type(16)));
typedef float f32x8 __attribute__((ext_vector_type(8)));
template <int O> DI f32x16 pv_mfma(bf16x8 v, const f32x16& p, f32x16 o, unsigned idx) {
  const f32x8 b = __builtin_shufflevector(p, p, O, O + 1, O + 2, O + 3, O + 4, O + 5, O + 6, O + 7);
  return __builtin_amdgcn_smfmac_f32_32x32x32_bf16(__builtin_bit_cast(bf16v8, v), __builtin_bit_cast(bf16v16, b), o, idx, 0, 0);
}
constexpr int ST_AUX = 16;
typedef __amdgpu_buffer_rsrc_t rsrc_t;
DI rsrc_t ws_rsrc(char* ws) { return __builtin_amdgcn_make_buffer_rsrc(ws, 0, WS_BYTES, 0x00020000); }
DI void st16(rsrc_t r, unsigned off, u32x4 v) { __builtin_amdgcn_raw_buffer_store_b128(v, r, off, 0, ST_AUX); }
DI void st16(rsrc_t r, unsigned off, bf16x8 v) { __builtin_amdgcn_raw_buffer_store_b128(__builtin_bit_cast(u32x4, v), r, off, 0, ST_AUX); }
DI void st8(rsrc_t r, unsigned off, u32x2 v, unsigned soff = 0) { __builtin_amdgcn_raw_buffer_store_b64(v, r, off, soff, ST_AUX); }

typedef unsigned long long u64;
DI u32x2 tag2(u64 t) { return u32x2{(unsigned)t, (unsigned)(t >> 32)}; }
constexpr unsigned SPIN_CAP = 1u << 20;
DI void wait_flags(rsrc_t r, unsigned off, u64 tag) {
#pragma unroll 1
  for (unsigned n = 0; n < SPIN_CAP; ++n) {
    const u32x2 v = __builtin_amdgcn_raw_buffer_load_b64(r, off, 0, 16);
    if (__builtin_amdgcn_ballot_w64(v[0] != (unsigned)tag || v[1] != (unsigned)(tag >> 32)) == 0) break;
    __builtin_amdgcn_s_sleep(2);
    asm volatile("" ::: "memory");
  }
  asm volatile("" ::: "memory");
}
DI void publish(int w, u32x4 srd, unsigned off, u32x2 tagv) {
  asm volatile("s_cmp_lg_u32 %0, 0\n\ts_cbranch_scc1 1f\n\ts_mov_b64 exec, 1\n\tbuffer_store_dwordx2 %3, off, %1, %2 sc1\n\ts_mov_b64 exec, -1\n1:"
               ::"s"(w), "s"(srd), "s"(off), "v"(tagv) : "memory", "scc");
}
DI u32x2 load_flag(rsrc_t r, unsigned off) { return __builtin_amdgcn_raw_buffer_load_b64(r, off, 0, 16); }
DI void check_flags(u32x2 v, bool mine, rsrc_t r, unsigned off, u64 tag) {
  if (__builtin_amdgcn_ballot_w64(v[0] != (unsigned)tag || v[1] != (unsigned)(tag >> 32)) != 0 && mine) wait_flags(r, off, tag);
  asm volatile("" ::: "memory");
}
#define IC(n) std::integral_constant<int, (n)>{}
#define PRIO(n) __builtin_amdgcn_s_setprio(n)
constexpr int LDS_BYTES = 163840;

constexpr int K1_SLOT = 8704, K1_NSLOT = 18, K1_WIN = 6 * K1_SLOT;
constexpr int K1_CS = K1_SLOT * K1_NSLOT;
static_assert(K1_CS + 2 * 3584 <= LDS_BYTES, "lds");
constexpr unsigned CODE_AHEAD = 28672;
constexpr unsigned OOB = 0x80000000u;
constexpr int PUB0 = 53, PUBA = 62, PUB1 = 72;
constexpr int ST_PUB = 4, ST_GET = 17;
constexpr int MID0 = PUB1 - 14;
constexpr int glu_start(int n) { return n < 16 ? 36 + (3 * n) / 4 : 61 + 2 * (n - 16); }
constexpr int WD_AT = 12;
template <bool ISV> struct K1Plan {
  static constexpr int PS = ISV ? 6 : 5, BU = 7 * PS, TOT = ISV ? BU : BU + 14, P0 = PS + 3, LB = 3, NG = ISV ? 28 : 84;
  static constexpr int need(int st) {
    return st < 7 ? PS * (st + 1) : st == 7 ? (ISV ? 0 : BU + 6) : st == 10 ? TOT : st == 14 ? 0 : -1;
  }
  static constexpr int bar_step(int st) { return st == 0 ? 0 : st <= 7 ? 4 * st - 1 : 4 * st - LB; }
  static constexpr int stores_at(int M) {
    return (M == ST_PUB || M == ST_GET || M == WD_AT) ? 1 : 0;
  }
  struct Tab { int virt[TOT + 1], before[NG + 1]; };
  static constexpr Tab make() {
    Tab t = {};
    int v = -1;
    for (int u = 0; u <= TOT; ++u) {
      const bool pair = u < BU ? (u % PS == 0 || u % PS == 2 || (ISV && u % PS == 4)) : ((u - BU) & 1) == 0;
      ++v; if (pair && v % K1_NSLOT == K1_NSLOT - 1) ++v;
      t.virt[u] = v;
    }
    int n = P0;
    for (int m = 0; m <= NG; ++m) {
      t.before[m] = n;
      const int sb = (m + 1) >> 2, consumed = sb < 7 ? PS * sb : BU;
      int q = m < 4 ? 2 : (m & 3) < PS - 4 ? 2 : 1;
      while (q-- > 0 && n < TOT && t.virt[consumed] + K1_NSLOT > t.virt[n]) ++n;
    }
    return t;
  }
  static constexpr Tab tab = make();
  static constexpr int slot(int u) { return (tab.virt[u] % K1_NSLOT) * K1_SLOT; }
  static constexpr int issued_before(int M) { return tab.before[M]; }
  static constexpr int allowed(int st) {
    int n = tab.before[bar_step(st)] - need(st) + (st == 0 ? (ISV ? 1 : 2) : 0);
    for (int m = 0; m < bar_step(st); ++m) if (tab.before[m + 1] >= need(st)) n += stores_at(m);
    return n;
  }
  static constexpr int xoff(int st) { return slot(PS * st); }
  static constexpr int woff(int st) { return st < 7 ? slot(PS * st + 2) : slot(BU + 2 * (st % 7)); }
  static constexpr int coff(int st) { return slot(PS * st + 4); }
};
template <int NEW, int OLD> DI unsigned rebase(unsigned& a) {
  if constexpr (NEW / K1_WIN != OLD / K1_WIN) { a += (NEW / K1_WIN - OLD / K1_WIN) * K1_WIN; PIN(a); }
  return a + NEW % K1_WIN;
}
DI f32x4 sum2(u32x4 sel, unsigned idx, bf16x8 f0, bf16x8 f1, f32x4 c) {
  typedef short s16x16 __attribute__((ext_vector_type(16)));
  const s16x16 b = __builtin_shufflevector(f0, f1, 0, 1, 2, 3, 4, 5, 6, 7, 8, 9, 10, 11, 12, 13, 14, 15);
  return __builtin_amdgcn_smfmac_f32_16x16x64_bf16(__builtin_bit_cast(bf16v8, sel), __builtin_bit_cast(bf16v16, b), c, idx, 0, 0);
}
DI void touch(u32x4 srd, unsigned voff, unsigned so) {
  asm volatile("s_mov_b32 m0, %3\n\ts_nop 0\n\tbuffer_load_dword %0, %1, %2 offen sc1 lds" ::"v"(voff), "s"(srd), "s"(so), "n"(K1_SLOT - 256) : "memory");
}
DI float ldsf(unsigned a) { return *(const LAS float*)(uintptr_t)a; }
DI void stsf(unsigned a, float v) { *(LAS float*)(uintptr_t)a = v; }

typedef u64 u64x8 __attribute__((ext_vector_type(8)));
template <bool ISV, class WARM, class MID>
DI void phase1(u32x4 xs, u32x4 wAs, u32x4 wCs, char* ws, unsigned xvoff, unsigned wvoff, unsigned cvoff, int rb, int cg, int w, u64& tag, WARM&& warm, MID&& mid) {
  typedef K1Plan<ISV> P;
  constexpr int pitch = ISV ? 3 * DM * 2 : FF * 2;
  static_assert(P::issued_before(ISV ? 23 : 31) == P::TOT && P::allowed(7) >= 0, "plan");
  const int lane = TIDX & 63;
  const int tt = w & 3, ch = w >> 2, r = lane & 31, h = lane >> 5;
  unsigned rvoff = lane < 56 ? (128 * rb + 8 * cg + w) * (DM * 2) + lane * 16 : OOB;
  unsigned tvoff = (28 * rb + 7 * tt + (lane & 7)) * pitch + cg * 128 + FF;
  constexpr int BOFF = ISV ? 256 : FF;
  auto issue = [&](auto U) {
    constexpr int u = U.value;
    constexpr unsigned slot = P::slot(u);
    if constexpr (u < P::BU) {
      constexpr int c = u / P::PS, k = u % P::PS;
      if constexpr (k < 2) dma_c<k * 64 * (DM * 2) + c * 128, slot>(xs, xvoff, w * 1024);
      else if constexpr (k < 4) dma_c<(64 * c + 32 * (k - 2)) * pitch, slot>(wAs, wvoff, w * (ISV ? 1088 : 1056));
      else if constexpr (ISV) dma_c<(64 * c + 32 * (k - 4)) * pitch + BOFF, slot>(wAs, wvoff, w * 1088);
      else dma_c<64 * c * (3 * DM * 2), slot>(wCs, cvoff, w * 1088);
    } else {
      dma_c<32 * (u - P::BU) * pitch + BOFF, slot>(wAs, wvoff, w * 1056);
    }
  };
  if constexpr (ISV) issue(IC(5));
  u32x4 xrow;
  asm volatile("buffer_load_dwordx4 %0, %1, %2, 0 offen" : "=v"(xrow) : "v"(rvoff), "s"(xs) : "memory");
  asm volatile("s_barrier" ::: "memory");
  unroll<P::P0 - P::PS>([&](auto U) { issue(IC(P::PS + U.value)); });
  u32x2 epoch;
  asm volatile("buffer_load_dwordx2 %0, off, %1, %2 sc1" : "=v"(epoch) : "s"(make_srd(ws, WS_BYTES)), "s"(WS_F) : "memory");
  if constexpr (!ISV) touch(wAs, tvoff, 0);
  SBAR();
  const int rot = tt ^ (2 * ch);
  const int g = (lane >> 4) & 1, qq = (lane & 15) >> 2, p = lane & 3;
  unsigned xa[4], wa[4], ca[4], va[4][4], vb[3][4][4], xb[3][4];
  {
    const int d1 = (rot & 1) ? -512 : 512, d2 = (rot & 2) ? -K1_SLOT : K1_SLOT, d3 = (rot & 2) ? -4 * 1088 : 4 * 1088;
    xa[0] = ((tt >> 1) * K1_SLOT + (tt & 1) * 4096) + r * 128 + (((2 * rot + h) ^ ((r >> 1) & 7)) << 4);
    xa[1] = xa[0] ^ 32; xa[2] = xa[0] ^ 64; xa[3] = xa[0] ^ 96;
    const unsigned c = h * 256 + p * 8 + ((rot & 1) * 512 + ch * 64);
    wa[0] = ((lane >> 2) & 7) * 1056 + c + (rot >> 1) * K1_SLOT;
    wa[1] = wa[0] + d1; wa[2] = wa[0] + d2; wa[3] = wa[2] + d1;
    ca[0] = qq * 1088 + g * 32 + c + (rot >> 1) * 4 * 1088;
    ca[1] = ca[0] + d1; ca[2] = ca[0] + d3; ca[3] = ca[2] + d1;
    if constexpr (ISV) {
#pragma unroll
      for (int t = 0; t < 4; ++t) {
        const int col = (28 * (4 * ch + t) + 32 * (cg - 14) + 16 * g + 4 * p) & 255;
        va[t][0] = (col >> 7) * 2 * K1_SLOT + (((col >> 6) & 1) * 4 + qq) * 1088 + (col & 63) * 2 + h * 256 + ((rot & 1) * 512 + (rot >> 1) * K1_SLOT);
        va[t][1] = va[t][0] + d1; va[t][2] = va[t][0] + d2; va[t][3] = va[t][2] + d1;
      }
    }
  }
  u32x4 sel2, sel1;
  unsigned sidx;
  {
    const unsigned c0 = (lane & 2) ? 0u : 0x3f803f80u, c1 = c0 ^ 0x3f803f80u;
    const bool on = h == ((lane >> 2) & 1);
    const unsigned a0 = on ? c0 : 0u, a1 = on ? c1 : 0u;
    sel2 = u32x4{a0, a0, a1, a1};
    sidx = (lane & 1) ? 0xEEEEEEEEu : 0x44444444u;
    const unsigned b0 = (lane & 3) == g ? 0x3f803f80u : 0u;
    sel1 = u32x4{b0, b0, b0, b0};
  }
#pragma unroll
  for (int i = 0; i < 4; ++i) {
    PIN(xa[i]);
    if constexpr (ISV) { PIN(va[0][i]); PIN(va[1][i]); PIN(va[2][i]); PIN(va[3][i]); } else { PIN(wa[i]); PIN(ca[i]); }
  }
  PIN(sel2); PIN(sidx);
  if constexpr (!ISV) PIN(sel1);
  unsigned pa, pr, hoff;
  constexpr int LB = P::LB;
  bf16x8 xf[28];
  bf16x8 wf[LB + 1][4];
  f32x16 acc[4];
  f32x4 cs2 = {}, cs1 = {};
  float rden = 0.f, den = 0.f, gsc = 0.f, qsc = 0.f;
  bf16x8 mfrag = {};
  u32x4 outp = {};
  int T, P0, shp, sp;
  unsigned qk_base, fown, qk_off[3];
  const rsrc_t wr = ws_rsrc(ws);
  const u32x4 wss = make_srd(ws, WS_BYTES);
  const f32x16 zero = {};

  float s1 = 0.f, s2 = 0.f;
  u32x2 stg;
  auto rowstat = [&](auto STEP) {
    constexpr int i = STEP.value;
    if constexpr (i < 4) {
      const float lo = asf(xrow[i] << 16), hi = asf(xrow[i] & 0xffff0000u);
      s1 += lo + hi; s2 = __builtin_fmaf(lo, lo, s2); s2 = __builtin_fmaf(hi, hi, s2);
    } else if constexpr (i < 10) {
      constexpr int ctl[6] = {0x111, 0x112, 0x114, 0x118, 0x142, 0x143}, msk[6] = {15, 15, 15, 15, 10, 12};
      s1 += asf(__builtin_amdgcn_update_dpp(0u, asu(s1), ctl[i - 4], msk[i - 4], 15, false));
      s2 += asf(__builtin_amdgcn_update_dpp(0u, asu(s2), ctl[i - 4], msk[i - 4], 15, false));
    } else if constexpr (i == 10) {
      s1 *= 1.f / DM;
      s2 = bfround(__builtin_fmaxf(s2 * (1.f / DM) - s1 * s1, 0.f));
    } else if constexpr (i == 11) {
      s2 = __builtin_amdgcn_sqrtf(bfround(s2 + 1e-5f));
    } else {
      s1 = asf(cvtpk(s1, s2));
    }
    PIN(s1); PIN(s2);
  };
  auto cs_put = [&](unsigned a, float c) { const float hi = bfround(c); stsf(a, asf(cvtpk(hi, c - hi))); };
  auto cs_load = [&](int tile) { return ldsfrag(pr + tile * 512); };
  auto correct = [&](const f32x16& a, const bf16x8& cf) {
    f32x16 o;
    if constexpr (ISV) o = MFMA(mfrag, cf, a);
    else o = MFMA(cf, mfrag, a);
    PIN(o);
    return o;
  };
  auto glu_op = [&](f32x16& a, int e, int op) {
    float t = a[e];
    if (op == 0) t = t * gsc;
    if (op == 1) t = __builtin_amdgcn_exp2f(t);
    if (op == 2) t = __builtin_fmaf(t, den, den);
    if (op == 3) t = __builtin_amdgcn_rcpf(t);
    if (op == 4) t = t * a[8 + e];
    if (op < 5) { PIN(t); a[e] = t; }
    if (op == 5 && (e & 1)) { unsigned u = cvtpk(a[e - 1], a[e]); PIN(u); outp[e >> 1] = u; }
  };
  auto store_h = [&](int half, int tl) {
    st16(wr, hoff + half * 57344 + tl * 1024, outp);
  };
  f32x16 rs;
  auto store_v = [&](const f32x16& a, int t) {
    const float one = r == 28 ? 1.f : 0.f;
    f32x16 v;
#pragma unroll
    for (int j = 0; j < 16; ++j) v[j] = __builtin_fmaf(a[j], rs[j], one);
    const unsigned dst = WS_KV + (((8 * (cg - 14) + 4 * ch + t) * 64 + T) * 4 + 2) * 1024 + lane * 16;
    st16(wr, dst, pack8(v, 0));
    st16(wr, dst + 1024, pack8(v, 8));
    SBAR();
  };
  const bool isk = cg >= 7;
  unsigned qk[8], qs[2] = {0u, 0u};
  auto qk_pack = [&](const f32x16& a, int j) {
    unsigned u = cvtpk(a[2 * j] * qsc, a[2 * j + 1] * qsc);
    PIN(u);
    qk[j] = u;
    qs[j & 1] = sp == (j >> 1) ? u : qs[j & 1];
    PIN(qs[j & 1]);
  };
  auto late = [&](auto STEP) {
    constexpr int i = STEP.value;
    int c = cg, b = rb;
    asm volatile("" : "+s"(c), "+s"(b));
    if constexpr (i == 0) { T = 4 * b + tt; pa = K1_CS + ch * 3584 + (lane & 15) * 16 + tt * 4; pr = K1_CS + ch * 3584 + r * 16; asm volatile("" : "+s"(T)); PIN(pa); PIN(pr); }
    if constexpr (i == 1 && !ISV) { P0 = 8 * (isk ? c - 7 : c) + 4 * ch; shp = ((P0 + 3) * 37) >> 8; sp = 7 * shp + 3 - P0; asm volatile("" : "+s"(P0), "+s"(shp), "+s"(sp)); }
    if constexpr (i == 2 && !ISV) {
      qk_base = (isk ? WS_KV + T * 4096 : WS_Q + T * 2048) + 16 * (isk ? ((r & 0x13) | ((r & 4) << 1) | ((r & 8) >> 1)) : r);
      PIN(qk_base);
    }
    if constexpr (i == 3) { fown = (b * 16 + c) * 128; asm volatile("" : "+s"(fown)); }
    if constexpr (i == 3 && !ISV) { hoff = WS_H + (T * 112 + 4 * c + 2 * ch) * 1024 + lane * 16; PIN(hoff); }
    if constexpr ((i == 4 || i == 5) && !ISV) {
      const unsigned P = P0 + 2 * (i - 4) + h, hp = mul24(P, 37u) >> 8, m = P - mul24(hp, 7u);
      unsigned t = qk_base + ((2 * hp + (m >> 2)) << (isk ? 18 : 17)) + (m & 3) * 512;
      PIN(t);
      qk_off[i - 4] = m == 3 ? OOB : t;
      PIN(qk_off[i - 4]);
    }
    if constexpr (i == 6 && !ISV) { qk_off[2] = sp < 4 ? qk_base + ((2 * shp + h) << (isk ? 18 : 17)) + 3 * 512 : OOB; PIN(qk_off[2]); }
  };
  auto qk_store = [&](int i) {
    if (i < 2) {
      const auto s0 = __builtin_amdgcn_permlane32_swap(qk[4 * i], qk[4 * i + 2], false, false);
      const auto s1 = __builtin_amdgcn_permlane32_swap(qk[4 * i + 1], qk[4 * i + 3], false, false);
      st16(wr, qk_off[i], u32x4{s0[0], s1[0], s0[1], s1[1]});
    } else {
      st16(wr, qk_off[2], u32x4{qs[0], qs[1], isk ? 0x3f80u : 0u, 0u});
    }
  };
  auto sync = [&](auto STG) {
    constexpr int st = STG.value;
    if constexpr (P::need(st) > 0) wait_vm_bar<P::allowed(st)>();
    else if constexpr (!ISV) wait_vm_bar<(glu_start(7) + 5 < PUB0) + (glu_start(15) + 5 < PUB0)>();
    else wait_lds_bar();
  };
  auto reads = [&](auto GG, auto PART) {
    constexpr int G = GG.value, st = G >> 2, i = G & 3, b = G % (LB + 1), ps = st > 0 ? st - 1 : 0, part = PART.value;
    if constexpr (G >= P::NG) {
    } else if constexpr (ISV) {
      constexpr int k = st % 3;
      static_assert(P::xoff(st) == k * K1_WIN && P::woff(st) == k * K1_WIN + 2 * K1_SLOT, "ring");
      if constexpr (st == 0) { vb[0][part][i] = va[part][i]; if constexpr (part == 0) xb[0][i] = xa[i]; }
      if constexpr (st == 1 || st == 2) {
        vb[k][part][i] = va[part][i] + k * K1_WIN; PIN(vb[k][part][i]);
        if constexpr (part == 0) { xb[k][i] = xa[i] + k * K1_WIN; PIN(xb[k][i]); }
      }
      if constexpr (part == 0) xf[G] = ldsfrag(xb[k][i]);
      wf[b][part] = trfrag(vb[k][part][i] + 2 * K1_SLOT, vb[k][part][i] + 2 * K1_SLOT + 128);
    } else if constexpr (st >= 7) {
      constexpr int o = P::woff(st), hi = o >= 3 * 2 * K1_SLOT;
      static_assert(o == (st % 7) * 2 * K1_SLOT, "second half");
      if constexpr (st == 7 && part == 0) { wa[i] -= (P::woff(6) / K1_WIN) * K1_WIN; PIN(wa[i]); }
      if constexpr (st == 10 && part == 0) { ca[i] = wa[i] + 3 * 2 * K1_SLOT; PIN(ca[i]); }
      const unsigned aw = hi ? ca[i] + (o - 3 * 2 * K1_SLOT) : wa[i] + o;
      if constexpr (part == 0) { if constexpr (st < 14) wf[b][0] = trfrag(aw, aw + 128); else wf[b][0] = trfrag(aw + 32, aw + 160); }
      if constexpr (part == 1 && st < 14 && i == 0) wf[b][1] = trfrag(aw + 32, aw + 160);
    } else if constexpr (part == 2) {
      const unsigned ac = rebase<P::coff(st), (st > 0 ? P::coff(ps) : 0)>(ca[i]);
      wf[b][2] = trfrag(ac, ac + 128);
    } else if constexpr (part == 0) {
      const unsigned aw = rebase<P::woff(st), (st > 0 ? P::woff(ps) : 0)>(wa[i]);
      xf[G] = ldsfrag(rebase<P::xoff(st), (st > 0 ? P::xoff(ps) : 0)>(xa[i]));
      wf[b][0] = trfrag(aw, aw + 128);
    } else {
      const unsigned aw = wa[i] + P::woff(st) % K1_WIN;
      wf[b][1] = trfrag(aw + 32, aw + 160);
    }
    SBAR();
  };
  auto mm = [&](f32x16& a, const f32x16& cin, const bf16x8& wfr, const bf16x8& xfr) {
    if constexpr (ISV) a = MFMA(xfr, wfr, cin); else a = MFMA(wfr, xfr, cin);
    PIN(a); SBAR();
  };
  bf16x8 cq[4];
  sync(IC(0));
  PIN(xrow);
  reads(IC(0), IC(0)); reads(IC(0), IC(1));
  reads(IC(0), IC(2));
  if constexpr (ISV) reads(IC(0), IC(3));
  unroll<P::NG>([&](auto MM) {
    constexpr int M = MM.value, pass = M / 28, ks = M % 28, b = M % (LB + 1), pos = M & 3;
    constexpr int n0 = P::issued_before(M), n1 = P::issued_before(M + 1);
    if constexpr (pass == 0) {
      mm(acc[0], ks == 0 ? zero : acc[0], wf[b][0], xf[ks]);
      if constexpr (pos == 3) sync(IC((M + 1) >> 2));
      reads(IC(M + 1), IC(0));
      if constexpr (M >= 5 && M < 13 && !ISV) warm(IC(M - 1));
      if constexpr (M >= 16 && M < 23) late(IC(M - 16));
      if constexpr (M <= ST_PUB) rowstat(IC(3 * M));
      if constexpr (M == 3) {
        static_assert(P::P0 < P::need(1), "epoch");
        PIN(epoch);
        tag = (((u64)__builtin_amdgcn_readfirstlane(epoch[1]) << 32) | (unsigned)__builtin_amdgcn_readfirstlane(epoch[0])) + 0x9E3779B97F4A7C15ull;
        asm volatile("" : "+s"(tag));
      }
      if constexpr (M == 23) {
        static_assert(P::issued_before(ST_GET + 1) < P::need(6), "statistics");
        PIN(stg);
        if (__builtin_amdgcn_ballot_w64(stg[0] != (unsigned)tag) != 0) {
#pragma unroll 1
          for (unsigned n = 0; n < SPIN_CAP; ++n) {
            stg = load_flag(wr, WS_ST + (32 * T + r) * 8);
            if (__builtin_amdgcn_ballot_w64(stg[0] != (unsigned)tag) == 0) break;
            __builtin_amdgcn_s_sleep(2);
            asm volatile("" ::: "memory");
          }
        }
      }
      if constexpr (M == 24) {
        den = asf(stg[1] & 0xffff0000u);
        rden = __builtin_amdgcn_rcpf(den);
        const unsigned mneg = h ? 0u : (stg[1] & 0xffffu) * 0x10001u ^ 0x80008000u;
        mfrag = __builtin_bit_cast(bf16x8, u32x4{mneg, mneg, mneg, mneg});
        PIN(mfrag); PIN(den); PIN(rden);
      }
      if constexpr (M == 25) {
        if constexpr (ISV) stsf(K1_CS + ch * 3584 + 2048 + (tt * 32 + r) * 4, rden);
        else { gsc = -LOG2E * rden; qsc = rden * (isk ? 1.f : QSCALE); PIN(gsc); PIN(qsc); }
      }
      if constexpr (M == 26 && ISV) {
#pragma unroll
        for (int a4 = 0; a4 < 4; ++a4) {
          const f32x4 t = *(const LAS f32x4*)(uintptr_t)((r < 28 ? K1_CS : 0x30000) + ch * 3584 + 2048 + (tt * 32 + 8 * a4 + 4 * h) * 4);
#pragma unroll
          for (int j = 0; j < 4; ++j) rs[4 * a4 + j] = t[j];
        }
      }
      if constexpr (M == 27) { cq[0] = cs_load(0); cq[1] = cs_load(1); cq[2] = cs_load(ISV ? 2 : 4); if constexpr (ISV) cq[3] = cs_load(3); }
      SBAR();
      mm(acc[1], ks == 0 ? zero : acc[1], wf[b][1], xf[ks]);
      reads(IC(M + 1), IC(1));
      if constexpr (M < ST_PUB) rowstat(IC(3 * M + 1));
      if constexpr (M == 27) { reads(IC(29), IC(0)); reads(IC(30), IC(0)); }
      if constexpr (M == 25) {
        cs_put(pa + g * 512, cs2[0] + cs2[1]); cs_put(pa + g * 512 + 256, cs2[2] + cs2[3]);
        if constexpr (!ISV) { cs_put(pa + 2048, cs1[0]); cs_put(pa + 2048 + 256, cs1[1]); }
        else { cs_put(pa + g * 512 + 1024, cs1[0] + cs1[1]); cs_put(pa + g * 512 + 1024 + 256, cs1[2] + cs1[3]); }
        cs2 = f32x4{};
      }
      if constexpr (n1 > n0 + 1) issue(IC(n0));
      SBAR();
      mm(acc[2], ks == 0 ? zero : acc[2], wf[b][2], xf[ks]);
      reads(IC(M + 1), IC(2));
      if constexpr (ISV) { mm(acc[3], ks == 0 ? zero : acc[3], wf[b][3], xf[ks]); reads(IC(M + 1), IC(3)); }
      if constexpr (n1 > n0) issue(IC(n1 > n0 ? n1 - 1 : 0));
      if constexpr (pos == 0) {
        cs2 = sum2(sel2, sidx, wf[b][0], wf[b][1], cs2); PIN(cs2);
        if constexpr (!ISV) { cs1 = __builtin_amdgcn_mfma_f32_16x16x32_bf16(__builtin_bit_cast(bf16x8, sel1), wf[b][2], cs1, 0, 0, 0); PIN(cs1); }
        else { cs1 = sum2(sel2, sidx, wf[b][2], wf[b][3], cs1); PIN(cs1); }
      }
      if constexpr (M < ST_PUB) rowstat(IC(3 * M + 2));
      if constexpr (M == WD_AT) warm(IC(3));
      if constexpr (M == ST_PUB) { if (lane == 63) st8(wr, 0, u32x2{(unsigned)tag, asu(s1)}, WS_ST + (128 * rb + 8 * cg + w) * 8); }
      if constexpr (M == ST_GET)
        asm volatile("buffer_load_dwordx2 %0, %1, %2, %3 offen sc1" : "=v"(stg) : "v"(r * 8), "s"(wss), "s"(WS_ST + T * 256) : "memory");
    } else {
      f32x16& a = acc[pass == 1 ? 3 : 2];
      if constexpr (M + LB < P::NG && ((M + LB) & 3) == 0 && P::need((M + LB) >> 2) >= 0) sync(IC((M + LB) >> 2));
      {
        constexpr int bs[6] = {27, 37, 53, PUBA + 1, PUB1 + 1, 84};
        constexpr int k = M < bs[1] ? 0 : M < bs[2] ? 1 : M < bs[3] ? 2 : M < bs[4] ? 3 : 4;
        constexpr int len = bs[k + 1] - bs[k], q = 4 * (M - bs[k]) / len, qp = M == bs[k] ? -1 : 4 * (M - 1 - bs[k]) / len;
        if constexpr (q != qp) PRIO(3 - q);
      }
      if constexpr (M == 56) a = correct(zero, cq[1]);
      mm(a, M == 28 ? zero : a, wf[b][0], xf[ks]);
      if constexpr (M == PUB0 && !ISV) {
        static_assert(PUB0 == P::bar_step(14), "publish");
        publish(w, wss, WS_F1 + fown, tag2(tag));
      }
      if constexpr (!ISV && M >= MID0) mid(IC(M - MID0));
      if constexpr (M == PUBA && !ISV) {
        static_assert(glu_start(15) + 5 < PUBA, "publish");
        wait_vm_rawbar<0>();
        publish(w, wss, WS_FA + fown, tag2(tag));
      }
      if constexpr (M + LB < P::NG) { reads(IC(M + LB < P::NG ? M + LB : 0), IC(0)); reads(IC(M + LB < P::NG ? M + LB : 0), IC(1)); }
      if constexpr (n1 > n0) issue(IC(n0));
      if constexpr (n1 > n0 + 1) issue(IC(n0 + 1));
      if constexpr (pass == 1 && pos == 0) { cs2 = sum2(sel2, sidx, wf[b][0], wf[b][1], cs2); PIN(cs2); }
      if constexpr (M == 28) acc[2] = correct(acc[2], cq[2]);
      if constexpr (M == 29) acc[0] = correct(acc[0], cq[0]);
      if constexpr (M == 30) acc[1] = correct(acc[1], cq[1]);
      if constexpr (M == 53) { cq[0] = cs_load(2); cq[1] = cs_load(3); }
      if constexpr (M == 57) acc[3] = correct(acc[3], cq[0]);
      {
        if constexpr (M >= 31 && M < 35) { qk_pack(acc[2], 2 * (M - 31)); qk_pack(acc[2], 2 * (M - 31) + 1); }
        if constexpr (M == 37) qk_store(0);
        if constexpr (M == 38) qk_store(1);
        if constexpr (M == 39) qk_store(2);
        unroll<24>([&](auto NN) {
          constexpr int n = NN.value, op = M - glu_start(n);
          if constexpr (op >= 0 && op < 6) {
            glu_op(acc[n < 8 ? 0 : n < 16 ? 1 : 3], n & 7, op);
            if constexpr (op == 5 && (n & 7) == 7) store_h(n >> 4, (n >> 3) & 1);
          }
        });
        static_assert(glu_start(15) + 5 < PUB1 && glu_start(23) + 5 >= PUB1 && glu_start(23) + 5 < 84, "glu");
      }
      if constexpr (M == 28 + 24) {
        cs_put(pa + g * 512 + 1024, cs2[0] + cs2[1]); cs_put(pa + g * 512 + 1024 + 256, cs2[2] + cs2[3]);
      }
    }
    SBAR();
  });
  if constexpr (ISV) {
#pragma unroll
    for (int t = 0; t < 4; ++t) acc[t] = correct(acc[t], cq[t]);
    store_v(acc[0], 0); unroll<4>([&](auto I) { warm(IC(4 + I.value)); }); SBAR();
    store_v(acc[1], 1); unroll<4>([&](auto I) { warm(IC(8 + I.value)); }); SBAR();
    store_v(acc[2], 2); unroll<6>([&](auto I) { mid(I); }); SBAR();
    store_v(acc[3], 3); unroll<PUB1 - MID0 - 6>([&](auto I) { mid(IC(6 + I.value)); });
    wait_vm_bar<0>();
    mid(IC(-1));
    publish(w, wss, WS_F1 + fown, tag2(tag));
    unroll<84 - PUB1>([&](auto I) { mid(IC(PUB1 - MID0 + I.value)); });
    wait_vm<0>();
    mid(IC(84 - MID0));
  } else {
    static_assert(glu_start(23) + 5 > PUB1 + 7, "one store behind the flags");
    wait_vm<1>();
    mid(IC(84 - MID0));
    SBAR();
#pragma unroll
    for (int op = 0; op < 6; ++op)
#pragma unroll
      for (int e = 0; e < 8; ++e) glu_op(acc[2], e, op);
    store_h(1, 1);
  }
}

template <int HI> DI f32x8 half8(const f32x16& v) {
  return __builtin_shufflevector(v, v, 8 * HI, 8 * HI + 1, 8 * HI + 2, 8 * HI + 3, 8 * HI + 4, 8 * HI + 5, 8 * HI + 6, 8 * HI + 7);
}
DI void put_half(unsigned a, const f32x8& v) {
  *(LAS f32x4*)(uintptr_t)a = __builtin_shufflevector(v, v, 0, 1, 2, 3);
  *(LAS f32x4*)(uintptr_t)(a + 1024) = __builtin_shufflevector(v, v, 4, 5, 6, 7);
}
DI void add_half(unsigned a, f32x8& v) {
  const f32x4 t0 = *(const LAS f32x4*)(uintptr_t)a, t1 = *(const LAS f32x4*)(uintptr_t)(a + 1024);
  v += __builtin_shufflevector(t0, t1, 0, 1, 2, 3, 4, 5, 6, 7);
}
DI float rowmax16(const f32x16& s) {
  float a = __builtin_fmaxf(__builtin_fmaxf(s[0], s[1]), s[2]), b = __builtin_fmaxf(__builtin_fmaxf(s[3], s[4]), s[5]);
  a = __builtin_fmaxf(__builtin_fmaxf(a, s[6]), s[7]); b = __builtin_fmaxf(__builtin_fmaxf(b, s[8]), s[9]);
  a = __builtin_fmaxf(__builtin_fmaxf(a, s[10]), s[11]); b = __builtin_fmaxf(__builtin_fmaxf(b, s[12]), s[13]);
  a = __builtin_fmaxf(__builtin_fmaxf(a, s[14]), s[15]);
  return swap32_max(__builtin_fmaxf(a, b));
}
constexpr int SLOT = 40960, GP = 16384;
__global__ __launch_bounds__(512, 2) void fused(const char*, const char*, const char*, const char*, const char*, const char*, char*, char*) {
  extern __shared__ __attribute__((aligned(16))) char smem[];
  u64x8 arg;
  asm volatile("s_load_dwordx16 %0, %1, 0x0" : "=s"(arg) : "s"(__builtin_amdgcn_kernarg_segment_ptr()) : "memory");
  const int xcd = BIDX & 7, idx = BIDX >> 3;
  const int w = __builtin_amdgcn_readfirstlane(TIDX >> 6);
  const int hi = idx >= 24 ? 4 : 0;
  const int rb = (xcd & 1) * 8 + ((idx & 7 & ~hi) | (xcd & hi));
  int cgl = 3 * (xcd >> 1) + (idx >> 3), cgh = 12 + ((xcd >> 1) & 1) + ((idx >> 1) & 2);
  asm volatile("" : "+s"(cgl), "+s"(cgh));
  const int cg = hi ? cgh : cgl;
  u64 pc;
  asm volatile("s_getpc_b64 %0" : "=s"(pc));
  touch(make_srd((const void*)pc, w >= 4 ? CODE_AHEAD : 0u), (TIDX & 63) << 7, ((idx + w) & 3) * 8192);
  const bool isv = cg >= 14;
  const int xr0 = 8 * w + ((TIDX & 63) >> 3), pitch = isv ? 3 * DM * 2 : FF * 2;
  unsigned xvoff = (128 * rb + xr0) * (DM * 2) + (((TIDX & 7) ^ ((xr0 >> 1) & 7)) << 4);
  int cov = 2 * DM * 2 - 14 * 384 + cg * 384 + (w >> 2) * 128;
  asm volatile("" : "+s"(cov));
  const unsigned wrow = ((TIDX & 63) >> 3) * 4 + (w & 3), wcol = ((TIDX & 7) << 4) + (isv ? cov : cg * 128);
  unsigned w1off = mul24(wrow, pitch) + wcol;
  unsigned w4off = mul24(wrow, 3 * DM * 2) + wcol + (isv ? 256 : (w >> 2) * 32 * (3 * DM * 2));
  unsigned m2 = w * (isv ? 1088 : 1056) + 2 * K1_SLOT, s3 = 32 * pitch;
  PIN(w4off);
  PIN(xvoff); PIN(w1off);
  asm volatile("s_waitcnt lgkmcnt(0)" : "+s"(arg), "+s"(m2), "+s"(s3));
  SBAR();
  const u32x4 xs = make_srd((const void*)arg[0], SEQ * DM * 2);
  dma_c<0, 0>(xs, xvoff, w * 1024);
  dma_c<64 * (DM * 2), K1_SLOT>(xs, xvoff, w * 1024);
  const u32x4 wAs = make_srd((const void*)(isv ? arg[1] : w >= 4 ? arg[4] : arg[3]), isv ? DM * 3 * DM * 2 : DM * FF * 2);
  dma_c<0, 0>(wAs, w1off, m2);
  asm volatile("s_add_u32 m0, %3, %4\n\ts_nop 0\n\tbuffer_load_dwordx4 %0, %1, %2 offen lds" ::"v"(w1off), "s"(wAs), "s"(s3), "s"(m2), "n"(K1_SLOT) : "memory", "scc");
  const u32x4 wCs = make_srd((const void*)arg[1], DM * 3 * DM * 2);
  dma_c<0, 4 * K1_SLOT>(wCs, w4off, w * 1088);
  u64 ptr[5] = {arg[0], arg[2], arg[5], arg[6], arg[7]};
  asm volatile("" : "+s"(ptr[0]), "+s"(ptr[1]), "+s"(ptr[2]), "+s"(ptr[3]), "+s"(ptr[4]));
  SBAR();
#define x ((const char*)ptr[0])
#define wo ((const char*)ptr[1])
#define wd ((const char*)ptr[2])
#define ws ((char*)ptr[3])
#define out ((char*)ptr[4])
#define wr ws_rsrc(ws)
  int head, qb, tp, np;
  unsigned kvb, hb;
  const bool live = !isv;
  const int lane = TIDX & 63, r = lane & 31, h = lane >> 5, qt = w & 3, kh = w >> 2;
  u64 tag;
  u32x4 wss, sA, sB, sC;
  unsigned l16, wvoff, vA0, vA1, vB0, vB1, vC, oA, oB, oC, dA, dB, dC;
  const unsigned mw = w * 4096, mc = 32768 + w * 1024;
  u32x4 hss, wds;
  unsigned spm, oQ, oS;
  auto warm = [&](auto STEP) {
    int c = BIDX;
    asm volatile("" : "+s"(c));
    if constexpr (STEP.value == 3) {
      const int c2 = c < 245 ? c : c - 245;
      touch(make_srd(c2 < 196 ? wd : wo, c2 < 196 ? FF * DM * 2 : DM * DM * 2), (lane & 7) << 7, (c2 < 196 ? c2 : c2 - 196) * 8192 + w * 1024);
    }
#define SPIN(v_) asm volatile("" : "+s"(v_))
    if constexpr (STEP.value == 4) { head = (c & 7) * 2 + (c >> 7); qb = (c >> 3) & 15; kvb = WS_KV + head * 64 * 4096; SPIN(head); SPIN(qb); SPIN(kvb); }
    if constexpr (STEP.value == 5) { const int gi = live ? c >> 3 : 0, q7 = (gi * 37) >> 8; tp = (c & 7) * 4 + q7; np = gi - 7 * q7; SPIN(tp); SPIN(np); }
    if constexpr (STEP.value == 6) { hb = WS_H + 2 * tp * 112 * 1024; wds = make_srd(wd + np * 128, live ? FF * DM * 2 - np * 128 : 0u); SPIN(hb); SPIN(wds); }
    if constexpr (STEP.value == 7) { unsigned t = kvb + ((w >> 1) * 32 + (w & 1)) * 4096 + 2048; SPIN(t); oA = kh ? qt * 16 * (DM * 2) : t; SPIN(oA); }
    if constexpr (STEP.value == 8) { oB = kh ? hb + qt * 1024 - 2048 : oA + 4096; dC = qt < 2 ? 96 * (DM * 2) : 6144; SPIN(oB); SPIN(dC); }
    if constexpr (STEP.value == 9) { oC = ((4 + kh) * 16 + qt * 8) * (DM * 2); SPIN(oC); }
    if constexpr (STEP.value == 10) { unsigned t = hb + ((qt - 2) * 112 + 4 + kh) * 1024; SPIN(t); oC = qt < 2 ? oC : t; SPIN(oC); }
    if constexpr (STEP.value == 11) { dA = kh ? 96 * (DM * 2) : 8192; dB = kh ? 6144 : 8192; spm = w == 0 ? 0x1011u : kh ? 0x10u : 0x1010u; SPIN(dA); SPIN(dB); SPIN(spm); }
  };
  u32x2 fl;
  unsigned foff;
  const f32x16 zero = {};
  unsigned pidx, gl, av, aw, ah, av2, aw2, ah2, aw0, ah0;
  u32x2 tagv;
  f32x16 o, ga, s[2];
  bf16x8 k0, k1, v0, v1, gw, gh, gw2, gh2;
  constexpr unsigned LDS_OOB = 0x30000;
  unsigned zaddr, oU, f2off;
  auto mid = [&](auto STEP) {
    constexpr int i = STEP.value, Y = PUB1 - MID0;
    if constexpr (i == 0) { foff = (28 * head + ((lane & 16) ? 27 : 0)) >> 6; PIN(foff); }
    if constexpr (i == 1) { foff += h ? qb * 16 : (lane & 15) * 16 + 7; PIN(foff); }
    if constexpr (i == 2) { foff = w == 0 ? WS_F1 + foff * 128 : OOB; l16 = lane * 16; PIN(foff); PIN(l16); }
    if constexpr (i == 3) { wvoff = (lane >> 3) * (DM * 2) + ((((lane >> 2) ^ (lane >> 4)) & 1) << 6) + (l16 & 48); PIN(wvoff); }
    if constexpr (i == 4) { gl = ((lane >> 2) & 3) * 128 + ((((lane >> 3) ^ qt) & 1) << 6); PIN(gl); }
    if constexpr (i == 5) { gl += (lane & 16) * 2 + (lane & 3) * 8; av = kh * 8192 + l16; PIN(gl); PIN(av); }
    if constexpr (i == 6) fl = load_flag(wr, foff);
    if constexpr (i == -1) PIN(fl);
    if constexpr (i == 6) { aw = GP + kh * 12288 + h * 512 + gl; ah = GP + kh * 12288 + 2048 + (qt >> 1) * 1024 + l16; PIN(aw); PIN(ah); }
    if constexpr (i == Y + 4) { av2 = av + 2 * SLOT; aw2 = aw + 2 * SLOT; ah2 = ah + 2 * SLOT; aw0 = LDS_OOB; ah0 = LDS_OOB; PIN(av2); PIN(aw2); PIN(ah2); PIN(aw0); PIN(ah0); }
    if constexpr (i == 7) { wss = make_srd(ws, WS_BYTES); hss = make_srd(ws, live ? WS_BYTES : 0u); SPIN(hss); vC = qt < 2 ? wvoff : l16; PIN(vC); }
    if constexpr (i == 8) { sC = qt < 2 ? wds : hss; SPIN(sC); vA0 = kh ? wvoff : l16; vB0 = l16; PIN(vA0); PIN(vB0); }
    if constexpr (i == 9) { sA = kh ? wds : wss; SPIN(sA); vA1 = kh ? wvoff + 8 * (DM * 2) - 1024 : l16; PIN(vA1); }
    if constexpr (i == 10) { sB = kh ? hss : wss; SPIN(sB); vB1 = kh ? l16 + 111 * 1024 : l16; PIN(vB1); }
    if constexpr (i == 11) { tagv = tag2(tag); pidx = 0xDDDDDDDDu; zaddr = LDS_OOB; PIN(tagv); PIN(pidx); PIN(zaddr); }
    if constexpr (i == 12) { oQ = WS_Q + (head * 64 + 4 * qb) * 2048 + w * 1024; SPIN(oQ); }
    if constexpr (i == 13) { oS = kvb + (kh * 32 + (qt >> 1)) * 4096 + (qt & 1) * 1024; SPIN(oS); }
    if constexpr (i == Y) {
      static_assert(((PUB1 + 2 - 56) >> 2) * 2 * K1_SLOT >= SLOT + GP && 14 * K1_SLOT <= 3 * SLOT, "phase 1 is done with these parts of LDS");
      check_flags(fl, w == 0, wr, foff, tag);
      wait_lds_bar();
      set_m0(3 * SLOT + w * 1024);
      dma_m0<0>(wss, l16, oQ);
    }
    if constexpr (i == Y + 1) { set_m0(3 * SLOT + 8192 + kh * 8192 + qt * 1024); dma_m0<0>(wss, l16, oS); }
    if constexpr (i == Y + 2) { oU = kvb + ((qt >> 1) * 32 + (qt & 1) + 2 * kh) * 4096 + 2048; SPIN(oU); set_m0(qt * 4096 + kh * SLOT); dma_m0<2048>(wss, l16, oU + 4096); }
    if constexpr (i == Y + 3) dma_m0<3072>(wss, l16, oU + 4096);
    if constexpr (i == Y + 5) { f2off = WS_F2 + ((tp >> 1) * 16 + ((lane & 15) < 14 ? (lane & 15) : 0)) * 128; PIN(f2off); }
    if constexpr (i == Y + 6) { foff = h ? WS_F1 + ((lane & 15) * 16 + 14 + (head >> 3)) * 128 : f2off - (WS_F2 - WS_FA); PIN(foff); }
    if constexpr (i == Y + 7) asm volatile("buffer_load_dwordx2 %0, %1, %2, 0 offen sc1" : "=v"(fl) : "v"(foff), "s"(wss) : "memory");
    if constexpr (i == Y + 12) {
      PIN(fl);
      check_flags(fl, true, wr, foff, tag);
      dma_m0<0>(wss, l16, oU); dma_m0<1024>(wss, l16, oU);
    }
    auto zfrag = [&]() { bf16x8 z = ldsfrag(zaddr); PIN(zaddr); return z; };
    auto ztile = [&]() { const f32x4 a = __builtin_bit_cast(f32x4, zfrag()), b = __builtin_bit_cast(f32x4, zfrag()), c = __builtin_bit_cast(f32x4, zfrag()), d = __builtin_bit_cast(f32x4, zfrag());
      return __builtin_shufflevector(__builtin_shufflevector(a, b, 0, 1, 2, 3, 4, 5, 6, 7), __builtin_shufflevector(c, d, 0, 1, 2, 3, 4, 5, 6, 7),
                                     0, 1, 2, 3, 4, 5, 6, 7, 8, 9, 10, 11, 12, 13, 14, 15); };
    if constexpr (i == Y + 8) { v0 = zfrag(); v1 = zfrag(); gw = zfrag(); }
    if constexpr (i == Y + 9) { o = ztile(); gh = zfrag(); }
    if constexpr (i == Y + 10) { ga = ztile(); gw2 = zfrag(); }
    if constexpr (i == Y + 11) { s[1] = ztile(); gh2 = zfrag(); }
  };
  if (__builtin_expect(cg < 14, 1)) phase1<false>(xs, wAs, wCs, ws, xvoff, w1off, w4off, rb, cg, w, tag, warm, mid);
  else phase1<true>(xs, wAs, wCs, ws, xvoff, w1off, w4off, rb, cg, w, tag, warm, mid);
  asm volatile("s_barrier" ::: "memory");
  const bf16x8 q0 = ldsfrag(3 * SLOT + qt * 2048 + l16);
  bf16x8 q1 = ldsfrag(3 * SLOT + qt * 2048 + 1024 + l16);
  {
    const unsigned st = av + 3 * SLOT + 8192;
    u32x4 qv = __builtin_bit_cast(u32x4, q1);
    const bf16x8 r0 = ldsfrag(3 * SLOT + 8192 + l16), r1 = ldsfrag(3 * SLOT + 8192 + 1024 + l16), a0 = ldsfrag(st), a1 = ldsfrag(st + 1024);
    k0 = ldsfrag(st + 2048); k1 = ldsfrag(st + 3072);
    SBAR();
    f32x16 t = MFMA(r0, q0, zero);
    t = MFMA(r1, q1, t);
    s[0] = MFMA(a0, q0, zero);
    PIN(t); PIN(s[0]); SBAR();
    if (kh) {
      set_m0(mw + SLOT);
      dma_m0<0>(sA, vA0, oA); dma_m0<1024>(sA, vA1, oA); dma_m0<2048>(sB, vB0, oB); dma_m0<3072>(sB, vB1, oB);
    }
    set_m0(mc + SLOT);
    dma_m0<0>(sC, vC, oC);
    SBAR();
    const float m = rowmax16(t);
    if (h) qv[2] = cvtpk(-m, 0.f);
    q1 = __builtin_bit_cast(bf16x8, qv);
    s[0] = MFMA(a1, q1, s[0]);
    PIN(k0); PIN(k1); PIN(s[0]); SBAR();
    if (!live) wait_vm<0>(); else if (!kh) wait_vm<2>();
  }
  const u32x4 wos = make_srd(wo + np * 128, DM * DM * 2 - np * 128);
  const unsigned wvoff2 = wvoff + 8 * (DM * 2) - 1024;
  {
    unsigned t = 0, tmp;
    u32x2 f2 = {};
    u32x4 sA2 = sA, sB2 = sB;
    unsigned a0 = vA0, a1 = vA1, b0 = vB0, b1 = vB1, pA = oA + (kh ? 0 : 8192), pB = oB + (kh ? 0 : 8192), pC = oC + dC, eA = dA, eB = dB;
    asm volatile(
      ".macro EXPS b, lo, hi\n .set e_, \\lo\n .rept \\hi - \\lo\n v_exp_f32 v[\\b + e_], v[\\b + e_]\n .set e_, e_ + 1\n .endr\n.endm\n"
      ".macro BAR on\n .if \\on\n s_waitcnt vmcnt(4)\n s_barrier\n .endif\n.endm\n"
      ".macro EPH b, bar\n EXPS \\b, 0, 2\n BAR \\bar\n s_setprio 1\n EXPS \\b, 2, 16\n.endm\n"
      ".macro GMM w, h, o, aw, ah\n v_mfma_f32_32x32x16_bf16 %[ga], v[\\w:\\w+3], \\h, %[ga]\n"
      " ds_read_b64_tr_b16 v[\\w:\\w+1], \\aw offset:\\o\n ds_read_b64_tr_b16 v[\\w+2:\\w+3], \\aw offset:\\o+1024\n ds_read_b128 \\h, \\ah offset:\\o\n.endm\n"
      ".macro ROUND kh, o, av, aw, ah, ws\n"
      " s_waitcnt lgkmcnt(0)\n"
      " v_smfmac_f32_32x32x32_bf16 %[o], %[v0], v[34:41], %[pidx]\n BAR 1-\\kh\n ds_read_b128 %[v0], \\av offset:\\o\n"
      " v_smfmac_f32_32x32x32_bf16 %[o], %[v1], v[42:49], %[pidx]\n ds_read_b128 %[v1], \\av offset:\\o+1024\n"
      " buffer_load_dwordx4 %[vC], %[sC], %[oC] offen sc1 lds\n"
      " GMM 90, %[gh], \\o, \\aw, \\ah\n"
      " v_mfma_f32_32x32x16_bf16 v[34:49], %[k0], %[q0], 0\n ds_read_b128 %[k0], \\av offset:\\o+2048\n s_setprio 2\n"
      " v_mfma_f32_32x32x16_bf16 v[34:49], %[k1], %[q1], v[34:49]\n ds_read_b128 %[k1], \\av offset:\\o+3072\n"
      " GMM 94, %[gh2], \\o+4096, \\aw, \\ah\n"
      " s_setprio 0\n s_add_u32 m0, %[mw], \\ws\n s_add_u32 %[oC], %[oC], %[dC]\n"
      " EPH 50, 0\n"
      " s_waitcnt lgkmcnt(0)\n"
      " v_smfmac_f32_32x32x32_bf16 %[o], %[v0], v[50:57], %[pidx]\n ds_read_b128 %[v0], \\av offset:\\o+4096\n"
      " buffer_load_dwordx4 %[vA0], s[68:71], %[oA] offen sc1 lds\n"
      " v_smfmac_f32_32x32x32_bf16 %[o], %[v1], v[58:65], %[pidx]\n ds_read_b128 %[v1], \\av offset:\\o+5120\n"
      " buffer_load_dwordx4 %[vA1], s[68:71], %[oA] offen offset:1024 sc1 lds\n"
      " GMM 90, %[gh], \\o+8192, \\aw, \\ah\n s_setprio 2\n"
      " v_mfma_f32_32x32x16_bf16 v[50:65], %[k0], %[q0], 0\n ds_read_b128 %[k0], \\av offset:\\o+6144\n"
      " buffer_load_dwordx4 %[vB0], s[72:75], %[oB] offen offset:2048 sc1 lds\n"
      " v_mfma_f32_32x32x16_bf16 v[50:65], %[k1], %[q1], v[50:65]\n ds_read_b128 %[k1], \\av offset:\\o+7168\n"
      " buffer_load_dwordx4 %[vB1], s[72:75], %[oB] offen offset:3072 sc1 lds\n"
      " s_setprio 0\n"
      ".endm\n"
      ".macro HK o\n s_add_u32 m0, %[mc], \\o\n s_add_u32 %[oA], %[oA], %[dA]\n s_add_u32 %[oB], %[oB], %[dB]\n.endm\n"
      ".macro LOOP kh\n"
      " HK 81920\n BAR \\kh\n s_branch 2f\n"
      "1:\n HK 81920\n s_cmp_lg_u32 %[t], 12\n s_cbranch_scc1 4f\n"
      "3:\n EPH 34, \\kh\n"
      "2:\n ROUND \\kh, 0, %[av], %[aw0], %[ah0], 81920\n HK 122880\n EPH 34, \\kh\n ROUND \\kh, 40960, %[av], %[aw], %[ah], 122880\n"
      " HK 0\n s_bitcmp1_b32 %[spm], %[t]\n s_cbranch_scc1 6f\n"
      "5:\n EPH 34, \\kh\n ROUND \\kh, 0, %[av2], %[aw2], %[ah2], 0\n HK 40960\n s_add_u32 %[t], %[t], 4\n EPH 34, \\kh\n ROUND \\kh, 40960, %[av2], %[aw2], %[ah2], 40960\n"
      " s_cmp_lt_u32 %[t], 16\n s_cbranch_scc1 1b\n s_branch 9f\n"
      "4:\n s_cmp_lg_u32 %[t], 4\n s_cbranch_scc1 11f\n v_mov_b32 %[aw0], %[aw]\n v_mov_b32 %[ah0], %[ah]\n s_branch 3b\n"
      "11:\n s_mov_b32 %[tmp], 0x100000\n"
      "12:\n v_cmp_ne_u64 vcc, %[f2], %[tag]\n s_cbranch_vccz 3b\n s_sleep 2\n buffer_load_dwordx2 %[f2], %[f2off], %[wss], 0 offen sc1\n"
      " s_waitcnt vmcnt(0)\n s_sub_u32 %[tmp], %[tmp], 1\n s_cbranch_scc0 12b\n s_branch 3b\n"
      "6:\n s_cmp_lg_u32 %[t], 4\n s_cbranch_scc1 7f\n buffer_load_dwordx2 %[f2], %[f2off], %[wss], 0 offen sc1\n s_branch 5b\n"
      "7:\n .if \\kh == 0\n s_cmp_lg_u32 %[t], 0\n s_cbranch_scc1 8f\n"
      " s_mov_b64 exec, 1\n buffer_store_dwordx2 %[tag], off, %[wss], %[f2own] sc1\n s_mov_b64 exec, -1\n s_branch 5b\n"
      "8:\n v_mov_b32 %[vA0], %[wv]\n v_mov_b32 %[vB0], %[wv]\n v_mov_b32 %[vA1], %[wv2]\n v_mov_b32 %[vB1], %[wv2]\n"
      " s_mov_b64 s[68:69], %[wolo]\n s_mov_b64 s[72:73], %[wolo]\n s_mov_b32 s70, %[won]\n s_mov_b32 s74, %[won]\n"
      " s_mov_b32 %[oA], %[woA]\n s_add_u32 %[oB], %[woA], 16 * 896 - 2048\n s_mov_b32 %[dA], 112 * 896\n s_mov_b32 %[dB], 112 * 896\n .endif\n s_branch 5b\n"
      ".endm\n"
      " s_cmp_eq_u32 %[kh], 0\n s_cbranch_scc1 10f\n LOOP 1\n"
      "10:\n LOOP 0\n"
      "9:\n EXPS 34, 0, 16\n s_waitcnt lgkmcnt(0)\n"
      " v_smfmac_f32_32x32x32_bf16 %[o], %[v0], v[34:41], %[pidx]\n s_nop 0\n v_smfmac_f32_32x32x32_bf16 %[o], %[v1], v[42:49], %[pidx]\n"
      " v_mfma_f32_32x32x16_bf16 %[ga], v[90:93], %[gh], %[ga]\n v_mfma_f32_32x32x16_bf16 %[ga], v[94:97], %[gh2], %[ga]\n s_nop 11\n"
      ".purgem EXPS\n.purgem BAR\n.purgem EPH\n.purgem GMM\n.purgem ROUND\n.purgem HK\n.purgem LOOP\n"
      : [o] "+v"(o), [ga] "+v"(ga), "+{v[34:49]}"(s[1]), "+{v[50:65]}"(s[0]), "+{v[90:93]}"(gw), "+{v[94:97]}"(gw2), [gh] "+v"(gh), [gh2] "+v"(gh2),
        [k0] "+v"(k0), [k1] "+v"(k1), [v0] "+v"(v0), [v1] "+v"(v1), [aw0] "+v"(aw0), [ah0] "+v"(ah0), [f2] "+v"(f2),
        [vA0] "+v"(a0), [vA1] "+v"(a1), [vB0] "+v"(b0), [vB1] "+v"(b1), "+{s[68:71]}"(sA2), "+{s[72:75]}"(sB2),
        [oA] "+s"(pA), [oB] "+s"(pB), [oC] "+s"(pC), [dA] "+s"(eA), [dB] "+s"(eB), [t] "+s"(t), [tmp] "=&s"(tmp)
      : [av] "v"(av), [aw] "v"(aw), [ah] "v"(ah), [av2] "v"(av2), [aw2] "v"(aw2), [ah2] "v"(ah2), [mw] "s"(mw), [mc] "s"(mc), [q0] "v"(q0), [q1] "v"(q1), [pidx] "v"(pidx), [vC] "v"(vC), [sC] "s"(sC), [dC] "s"(dC), [spm] "s"(spm), [kh] "s"(kh),
        [tag] "v"(tagv), [wss] "s"(wss), [f2own] "s"(WS_F2 + (rb * 16 + cg) * 128), [f2off] "v"(f2off), [wv] "v"(wvoff), [wv2] "v"(wvoff2),
        [wolo] "s"(((u64)wos[1] << 32) | wos[0]), [won] "s"(wos[2]), [woA] "s"(w * 28 * (DM * 2))
      : "memory", "scc", "vcc");
  }
  constexpr int MO = 2 * SLOT, WO2 = 2 * SLOT, KT = 3 * SLOT;
  f32x8 oh;
  const float ol = o[12];
  if (kh) { put_half(MO + w * 2048 + l16, half8<0>(o)); stsf(MO + 16384 + qt * 256 + lane * 4, ol); oh = half8<1>(o); }
  else { put_half(MO + w * 2048 + l16, half8<1>(o)); oh = half8<0>(o); }
  PRIO(0);
  wait_lds_bar();
  {
    const float lp = ldsf(kh ? MO + qt * 2048 + 1024 + l16 : MO + 16384 + qt * 256 + lane * 4);
    add_half(MO + (w ^ 4) * 2048 + l16, oh);
    float l = upper_half(ol + lp);
    float rl = __builtin_amdgcn_rcpf(l);
    float chk = (l > 1e-18f && l < 1e30f) ? 0.f : 1.f;
#pragma unroll
    for (int i = 0; i < 8; ++i) { oh[i] *= rl; chk = __builtin_fmaf(oh[i], 0.f, chk); }
    if (__builtin_expect(__builtin_amdgcn_ballot_w64(!(chk == 0.f)) != 0, 0)) {
      u32x4 qv = __builtin_bit_cast(u32x4, q1);
      if (h) qv[2] = 0u;
      q1 = __builtin_bit_cast(bf16x8, qv);
      float m = -3.0e38f;
      o = zero;
#pragma unroll 1
      for (int t = 0; t < 64; ++t) {
        const unsigned tb = kvb + t * 4096;
        k0 = __builtin_bit_cast(bf16x8, __builtin_amdgcn_raw_buffer_load_b128(wr, l16, tb, 16));
        k1 = __builtin_bit_cast(bf16x8, __builtin_amdgcn_raw_buffer_load_b128(wr, l16 + 1024, tb, 16));
        v0 = __builtin_bit_cast(bf16x8, __builtin_amdgcn_raw_buffer_load_b128(wr, l16 + 2048, tb, 16));
        v1 = __builtin_bit_cast(bf16x8, __builtin_amdgcn_raw_buffer_load_b128(wr, l16 + 3072, tb, 16));
        f32x16 u = MFMA(k0, q0, zero);
        u = MFMA(k1, q1, u);
        const float mn = __builtin_fmaxf(m, rowmax16(u));
        const float al = __builtin_amdgcn_exp2f(m - mn);
        m = mn;
#pragma unroll
        for (int i = 0; i < 16; ++i) { o[i] *= al; u[i] = __builtin_amdgcn_exp2f(u[i] - mn); }
        o = pv_mfma<0>(v0, u, o, pidx);
        o = pv_mfma<8>(v1, u, o, pidx);
      }
      rl = __builtin_amdgcn_rcpf(upper_half(o[12]));
#pragma unroll
      for (int i = 0; i < 8; ++i) oh[i] = (kh ? o[8 + i] : o[i]) * rl;
    }
    if (h && kh) oh[4] = 0.f;
    st16(wr, WS_O + ((4 * qb + qt) * 16 + head) * 2048 + kh * 1024 + l16,
         u32x4{cvtpk(oh[0], oh[1]), cvtpk(oh[2], oh[3]), cvtpk(oh[4], oh[5]), cvtpk(oh[6], oh[7])});
  }
  SBAR();
  set_m0(KT + w * 4096);
  dma_m0<0>(wds, wvoff, (102 + w) * 16 * (DM * 2)); dma_m0<1024>(wds, wvoff2, (102 + w) * 16 * (DM * 2));
  dma_m0<2048>(hss, l16, hb + (102 + w) * 1024 - 2048); dma_m0<3072>(hss, l16, hb + (102 + 111 + w) * 1024 - 2048);
  set_m0(KT + 32768 + w * 1024);
  dma_m0<0>(sC, vC, qt < 2 ? ((110 + kh) * 16 + qt * 8) * (DM * 2) : hb + ((qt - 2) * 112 + 110 + kh) * 1024);
  if (live) wait_vm<5>(); else wait_vm<0>();
  if (lane == 0) st8(wr, WS_F3 + (((4 * qb + qt) * 16 + head) * 2 + kh) * 128, tag2(tag));
  if (!live) {
    if (BIDX == 255 && w == 0) {
#pragma unroll 1
      for (int i = 0; i < 4; ++i) wait_flags(wr, WS_F1 + (i * 64 + lane) * 128, tag);
      if (lane == 0) st8(wr, WS_F, tag2(tag));
    }
    return;
  }
  wait_vm_bar<6>();
  {
    const unsigned so = (8 + w) * 28 * (DM * 2);
    set_m0(WO2 + w * 4096);
    dma_m0<0>(wos, wvoff, so); dma_m0<1024>(wos, wvoff2, so);
    dma_m0<2048>(wos, wvoff, so + 16 * (DM * 2) - 2048); dma_m0<3072>(wos, wvoff2, so + 16 * (DM * 2) - 2048);
  }
  unsigned yo = (64 * tp + 8 * w + (lane >> 3)) * (DM * 2) + np * 128 + (lane & 7) * 16;
  u32x4 xv;
  asm volatile("buffer_load_dwordx4 %0, %1, %2, 0 offen" : "=v"(xv) : "v"(yo), "s"(make_srd(x, SEQ * DM * 2)) : "memory");
  const unsigned oa = (qt >> 1) * 8192 + kh * 1024 + l16, wa = kh * 2048 + h * 512 + gl;
  bf16x8 wof[16];
  foff = w == 0 ? WS_F3 + (tp * 64 + lane) * 128 : OOB;
  asm volatile("buffer_load_dwordx2 %0, %1, %2, 0 offen sc1" : "=v"(fl) : "v"(foff), "s"(wss) : "memory");
  bf16x8 tw[6], th[6];
#pragma unroll
  for (int i = 0; i < 6; ++i) { const unsigned x_ = (i / 3) * SLOT + (i % 3) * 4096; tw[i] = trfrag(aw + x_, aw + x_ + 1024); th[i] = ldsfrag(ah + x_); }
#pragma unroll
  for (int i = 0; i < 8; ++i) { const unsigned a = wa + (i >> 2) * SLOT + (i & 3) * 4096; wof[i] = trfrag(a, a + 1024); }
  SBAR();
#pragma unroll
  for (int i = 0; i < 6; ++i) ga = MFMA(tw[i], th[i], ga);
  PIN(ga);
  if (w == 0) { asm volatile("s_waitcnt vmcnt(0)" : "+v"(fl)::"memory"); check_flags(fl, true, wr, foff, tag); }
  wait_vm_bar<6>();
  {
    const unsigned so = WS_O + (2 * tp + kh) * 32768 + qt * 2048;
    const unsigned bw = aw + KT - GP + kh * 8192, bh = ah + KT - GP + kh * 8192;
#pragma unroll
    for (int i = 0; i < 5; ++i) { tw[i] = trfrag(bw + i * 4096, bw + i * 4096 + 1024); th[i] = ldsfrag(bh + i * 4096); }
    SBAR();
#pragma unroll
    for (int c = 0; c < 4; ++c) {
      set_m0(c * 16384 + w * 2048); dma_m0<0>(wss, l16, so + c * 8192); dma_m0<1024>(wss, l16, so + c * 8192);
      ga = MFMA(tw[c], th[c], ga); PIN(ga); SBAR();
    }
    ga = MFMA(tw[4], th[4], ga);
  }
  f32x8 gh8;
  if (kh) { put_half(65536 + w * 2048 + l16, half8<0>(ga)); gh8 = half8<1>(ga); } else { put_half(65536 + w * 2048 + l16, half8<1>(ga)); gh8 = half8<0>(ga); }
  f32x16 sa = zero;
  unsigned ys = 65536 + (32 * (qt >> 1) + r) * 144 + (qt & 1) * 64 + kh * 32 + h * 8, yr = 65536 + (8 * w + (lane >> 3)) * 144 + (lane & 7) * 16;
  unsigned ms = KT + w * 2048 + l16, mr = KT + (w ^ 4) * 2048 + l16;
  unsigned wa2 = wa + WO2;
  PIN(ys); PIN(yr); PIN(yo); PIN(ms); PIN(mr); PIN(wa2);
  f32x8 xr;
  unroll<2>([&](auto C) {
    constexpr int c = C.value;
    if constexpr (c == 0) asm volatile("s_waitcnt vmcnt(4) lgkmcnt(0)\n\ts_barrier" : "+v"(xv), "+v"(fl)::"memory");
    else wait_vm_rawbar<0>();
    bf16x8 of[8];
    if constexpr (c == 0) add_half(65536 + (w ^ 4) * 2048 + l16, gh8);
#pragma unroll
    for (int j = 0; j < 8; ++j) of[j] = ldsfrag(oa + (2 * c + (j >> 2)) * 16384 + (j & 3) * 2048);
    SBAR();
#pragma unroll
    for (int j = 0; j < 8; ++j) {
      sa = MFMA(wof[8 * c + j], of[j], sa); PIN(sa);
      if constexpr (c == 0) {
        wof[8 + j] = trfrag(wa2 + j * 4096, wa2 + j * 4096 + 1024);
        float e = __builtin_amdgcn_exp2f(-LOG2E * gh8[j]) + 1.f; PIN(e); xr[j] = e;
      } else {
        float gq = gh8[j] * __builtin_amdgcn_rcpf(xr[j]); PIN(gq); gh8[j] = gq;
        const unsigned xw = xv[j >> 1];
        float t = asf((j & 1) ? (xw & 0xffff0000u) : (xw << 16)); PIN(t); xr[j] = t;
      }
      SBAR();
    }
  });
  f32x8 sh8;
  if (kh) { put_half(ms, half8<0>(sa)); sh8 = half8<1>(sa); } else { put_half(ms, half8<1>(sa)); sh8 = half8<0>(sa); }
  wait_lds_bar();
  add_half(mr, sh8);
  sh8 *= gh8;
  *(LAS u32x2*)(uintptr_t)ys = u32x2{cvtpk(sh8[0], sh8[1]), cvtpk(sh8[2], sh8[3])};
  *(LAS u32x2*)(uintptr_t)(ys + 16) = u32x2{cvtpk(sh8[4], sh8[5]), cvtpk(sh8[6], sh8[7])};
  wait_lds_bar();
  u32x4 y = *(const LAS u32x4*)(uintptr_t)yr;
#pragma unroll
  for (int i = 0; i < 4; ++i) { const unsigned u = y[i]; y[i] = cvtpk(xr[2 * i] + asf(u << 16), xr[2 * i + 1] + asf(u & 0xffff0000u)); }
  __builtin_amdgcn_raw_buffer_store_b128(y, __builtin_amdgcn_make_buffer_rsrc(out, 0, SEQ * DM * 2, 0x00020000), yo, 0, 0);
}

#undef x
#undef wo
#undef wd
#undef ws
#undef out
#undef wr
#if defined(__HIP_DEVICE_COMPILE__)
#pragma clang attribute pop
#endif

extern "C" void kernel_launch(void* const* d_in, const int* in_sizes, int n_in, void* d_out, int out_size, void* d_ws, size_t ws_size,
                              hipStream_t stream) {
  (void)hipFuncSetAttribute((const void*)fused, hipFuncAttributeMaxDynamicSharedMemorySize, LDS_BYTES);
  hipLaunchKernelGGL(fused, dim3(256), dim3(512), LDS_BYTES, stream, (const char*)d_in[0], (const char*)d_in[1], (const char*)d_in[2],
                     (const char*)d_in[4], (const char*)d_in[5], (const char*)d_in[6], (char*)d_ws, (char*)d_out);
}
```
